```python
import torch

def _softmax(x, axis=-1):
    return torch.softmax(x, dim=axis)
def _gelu(x):
    return torch.nn.functional.gelu(x, approximate="tanh")
def _silu(x):
    return torch.nn.functional.silu(x)
def _sigmoid(x):
    return torch.sigmoid(x)
def _layernorm(x, axis=-1, eps=1e-5):
    m = x.mean(dim=axis, keepdim=True)
    v = x.var(dim=axis, keepdim=True, unbiased=False)
    return (x - m) / torch.sqrt(v + eps)
def _scatter_add(tgt, idx, src, axis=0):
    return tgt.clone().index_add_(axis, idx, src)
def _segment_sum(src, seg, n):
    out = torch.zeros((n,) + src.shape[1:], dtype=src.dtype)
    return out.index_add_(0, seg, src)
def _pad(x, pw):
    flat = tuple(v for pair in reversed(pw) for v in pair)
    return torch.nn.functional.pad(x, flat)

kernel_name = 'gather_scatter__mpnn_edge_mlp__graph'

def setup_inputs():

    _w = (lambda a: (lambda t: t.to(torch.bfloat16) if t.is_floating_point() else t)(torch.as_tensor(a)))
    N, deg, D, De = 10000, 16, 128, max(4, 1)
    E = N * deg
    return {
        "x": _w((lambda *s: torch.randn(*s, dtype=torch.bfloat16) * 0.05)(N, D)),
        "e_attr": _w((lambda *s: torch.randn(*s, dtype=torch.bfloat16) * 0.4)(E, De)),
        "src": _w((lambda lo, hi, *s: torch.randint(lo, hi, s))(0, N, E).to(torch.int64)),
        "dst": _w((lambda lo, hi, *s: torch.randint(lo, hi, s))(0, N, E).to(torch.int64)),
        "w_msg": _w((lambda *s: torch.randn(*s, dtype=torch.bfloat16) * 0.4)(2 * D + De, D)),
        "w_upd": _w((lambda *s: torch.randn(*s, dtype=torch.bfloat16) * 0.05)(2 * D, D)),
    }

def reference(x, e_attr, src, dst, w_msg, w_upd):
    N, D = x.shape
    m_in = torch.concatenate([x[src], x[dst], e_attr], axis=-1)
    msg = _silu(m_in @ w_msg)
    agg = _segment_sum(msg, dst, N)
    return _silu(torch.concatenate([x, agg], axis=-1) @ w_upd)
```

```cpp
#include <hip/hip_runtime.h>
#include <hip/hip_bf16.h>
#include <stdint.h>

namespace {

typedef __attribute__((ext_vector_type(8))) __bf16 bf16x8;
typedef __attribute__((ext_vector_type(4))) __bf16 bf16x4;
typedef __attribute__((ext_vector_type(2))) __bf16 bf16x2;
typedef __attribute__((ext_vector_type(4))) short s16x4;
typedef __attribute__((ext_vector_type(16))) float f32x16;
typedef __attribute__((ext_vector_type(4))) float f32x4;
typedef __attribute__((ext_vector_type(2))) float f32x2;
typedef __attribute__((ext_vector_type(4))) unsigned u32x4;
typedef __attribute__((ext_vector_type(3))) unsigned u32x3;
typedef __attribute__((ext_vector_type(2))) unsigned u32x2;
#define LDS_AS __attribute__((address_space(3)))
#define DI __device__ __forceinline__

constexpr int NN = 10000, NE = 160000;
constexpr int NPROD = 256, CHUNK = 625;
constexpr int PTILES = (NN + 31) / 32;
constexpr int SUBN = 20;
constexpr float NEG_LOG2E = -1.4426950408889634f, NEG_LN2 = -0.6931471805599453f;

constexpr size_t WS_P = 0;
constexpr unsigned P_BYTES = PTILES * 32 * 256;
constexpr size_t WS_REC = WS_P + P_BYTES;
constexpr size_t WS_TAB = WS_REC + (size_t)NE * 12;
constexpr size_t WS_WF = WS_TAB + NPROD * 256 * 4;
constexpr size_t WS_W3 = WS_WF + 3 * 32768;

constexpr int K1_SORT0 = PTILES, K1_WT0 = K1_SORT0 + NPROD, K1_GRID = K1_WT0 + 25;

DI f32x16 mfma(bf16x8 a, bf16x8 b, f32x16 c) { return __builtin_amdgcn_mfma_f32_32x32x16_bf16(a, b, c, 0, 0, 0); }
DI unsigned pack2(float a, float b) {
  bf16x2 v = {(__bf16)a, (__bf16)b};
  return __builtin_bit_cast(unsigned, v);
}
DI bf16x8 as_frag(u32x4 v) { return __builtin_bit_cast(bf16x8, v); }
DI float bf2f(unsigned short u) { return __uint_as_float((unsigned)u << 16); }
DI unsigned swap23(unsigned v) { return (v & ~12u) | ((v & 4u) << 1) | ((v & 8u) >> 1); }
DI unsigned wave_scan(unsigned v) {
  v += __builtin_amdgcn_update_dpp(0u, v, 0x111, 0xf, 0xf, false);
  v += __builtin_amdgcn_update_dpp(0u, v, 0x112, 0xf, 0xf, false);
  v += __builtin_amdgcn_update_dpp(0u, v, 0x114, 0xf, 0xf, false);
  v += __builtin_amdgcn_update_dpp(0u, v, 0x118, 0xf, 0xf, false);
  v += __builtin_amdgcn_update_dpp(0u, v, 0x142, 0xa, 0xf, false);
  v += __builtin_amdgcn_update_dpp(0u, v, 0x143, 0xc, 0xf, false);
  return v;
}
DI bf16x8 tr_frag(const char* lo, const char* hi) {
  s16x4 a = __builtin_amdgcn_ds_read_tr16_b64_v4i16((LDS_AS s16x4*)lo);
  s16x4 b = __builtin_amdgcn_ds_read_tr16_b64_v4i16((LDS_AS s16x4*)hi);
  return __builtin_bit_cast(bf16x8, __builtin_shufflevector(a, b, 0, 1, 2, 3, 4, 5, 6, 7));
}

__global__ __launch_bounds__(256) void mpnn_prep(const unsigned short* __restrict__ x, const u32x2* __restrict__ eattr,
                                                 const int* __restrict__ src, const int* __restrict__ dst,
                                                 const unsigned short* __restrict__ wmsg,
                                                 const unsigned short* __restrict__ wupd, char* __restrict__ ws) {
  __shared__ __attribute__((aligned(16))) char lds[32768];
  const int tid = threadIdx.x, lane = tid & 63, w = __builtin_amdgcn_readfirstlane(tid >> 6);
  const int b = blockIdx.x;
  if (b < K1_SORT0) {
    char* img = lds + w * 8192;
#pragma unroll
    for (int i = 0; i < 8; ++i) {
      const int row = 16 * i + (lane >> 2), ch = (lane & 3) ^ ((lane >> 4) & 3);
      __builtin_amdgcn_global_load_lds((const unsigned*)(wmsg + row * 128 + w * 32 + ch * 8),
                                       (LDS_AS unsigned*)(img + i * 1024), 16, 0, 0);
    }
    const int c = lane & 31, h = lane >> 5, node = b * 32 + c;
    const u32x4* xr = (const u32x4*)(x + (size_t)min(node, NN - 1) * 128) + h;
    u32x4 xf[8];
#pragma unroll
    for (int s = 0; s < 8; ++s) xf[s] = xr[2 * s];
    asm volatile("s_waitcnt vmcnt(0)" ::: "memory");
    const int blk = (lane >> 4) & 1, q = (lane & 15) >> 2, p = lane & 3;
    f32x16 acc = {};
#pragma unroll
    for (int s = 0; s < 8; ++s) {
      const int r0 = 16 * s + 8 * h + q, r1 = r0 + 4, ch = 2 * blk + (p >> 1);
      const bf16x8 a = tr_frag(img + 64 * r0 + 16 * (ch ^ ((r0 >> 2) & 3)) + 8 * (p & 1),
                               img + 64 * r1 + 16 * (ch ^ ((r1 >> 2) & 3)) + 8 * (p & 1));
      acc = mfma(a, as_frag(xf[s]), acc);
    }
    u32x4 o0, o1;
#pragma unroll
    for (int i = 0; i < 4; ++i) {
      o0[i] = pack2(acc[2 * i] * NEG_LOG2E, acc[2 * i + 1] * NEG_LOG2E);
      o1[i] = pack2(acc[8 + 2 * i] * NEG_LOG2E, acc[9 + 2 * i] * NEG_LOG2E);
    }
    u32x4* po = (u32x4*)(ws + WS_P + (size_t)node * 256 + w * 64 + h * 32);
    po[0] = o0;
    po[1] = o1;
  } else if (b < K1_WT0) {
    const int pc = b - K1_SORT0, e0 = pc * CHUNK;
    unsigned* hist = (unsigned*)lds;
    unsigned* start = hist + 512 + w * 512;
    int dv[3], sv[3];
    u32x2 ev[3];
#pragma unroll
    for (int i = 0; i < 3; ++i) {
      const int k = min(tid + 256 * i, CHUNK - 1);
      dv[i] = dst[e0 + k];
      sv[i] = src[e0 + k];
      ev[i] = eattr[e0 + k];
    }
    hist[tid] = 0;
    hist[tid + 256] = 0;
    __syncthreads();
    unsigned key[3], rank[3], col[3];
#pragma unroll
    for (int i = 0; i < 3; ++i) {
      const unsigned d = dv[i], g = d & 255u, j = d >> 8, sub = j >= SUBN;
      key[i] = (((g & 7u) << 5) + (g >> 3)) * 2 + sub;
      col[i] = swap23(j - SUBN * sub);
      rank[i] = 0;
      if (tid + 256 * i < CHUNK) rank[i] = atomicAdd(&hist[key[i]], 1u);
    }
    __syncthreads();
    const u32x4 c0 = *(const u32x4*)(hist + 8 * lane), c1 = *(const u32x4*)(hist + 8 * lane + 4);
    const unsigned tot = c0[0] + c0[1] + c0[2] + c0[3] + c1[0] + c1[1] + c1[2] + c1[3];
    u32x4 s0, s1;
    s0[0] = wave_scan(tot) - tot;
    s0[1] = s0[0] + c0[0];
    s0[2] = s0[1] + c0[1];
    s0[3] = s0[2] + c0[2];
    s1[0] = s0[3] + c0[3];
    s1[1] = s1[0] + c1[0];
    s1[2] = s1[1] + c1[1];
    s1[3] = s1[2] + c1[2];
    *(u32x4*)(start + 8 * lane) = s0;
    *(u32x4*)(start + 8 * lane + 4) = s1;
    if (w == 0) {
      u32x4 t;
      t[0] = s0[0] | (s0[1] << 10) | (s0[2] << 20);
      t[1] = s0[2] | (s0[3] << 10) | (s1[0] << 20);
      t[2] = s1[0] | (s1[1] << 10) | (s1[2] << 20);
      t[3] = s1[2] | (s1[3] << 10) | ((s1[3] + c1[3]) << 20);
      *(u32x4*)(ws + WS_TAB + pc * 1024 + lane * 16) = t;
    }
    __builtin_amdgcn_wave_barrier();
#pragma unroll
    for (int i = 0; i < 3; ++i) {
      if (tid + 256 * i < CHUNK) {
        const unsigned pos = start[key[i]] + rank[i];
        u32x3 r = {((unsigned)sv[i] << 8) | col[i], ev[i][0], ev[i][1]};
        *(u32x3*)(ws + WS_REC + (size_t)(e0 + pos) * 12) = r;
      }
    }
  } else if (b < K1_GRID - 1) {
    const int t = (b - K1_WT0) * 256 + tid, mat = t >> 11, fb = (t >> 9) & 3, s = (t >> 6) & 7;
    const int c = lane & 31, h = lane >> 5;
    const unsigned short* wp = (mat == 0 ? wmsg + 128 * 128 : wupd + (mat - 1) * 128 * 128) + fb * 32 + c;
    unsigned v[8];
#pragma unroll
    for (int j = 0; j < 8; ++j) {
      const int k = mat == 2 ? 16 * s + 8 * (j >> 2) + 4 * h + (j & 3) : 16 * s + 8 * h + j;
      v[j] = wp[k * 128];
    }
    u32x4 o = {v[0] | (v[1] << 16), v[2] | (v[3] << 16), v[4] | (v[5] << 16), v[6] | (v[7] << 16)};
    *(u32x4*)(ws + WS_WF + (size_t)t * 16) = o;
  } else if (tid < 128) {
    float hi[4], lo[4];
#pragma unroll
    for (int k = 0; k < 4; ++k) {
      const float v = bf2f(wmsg[(256 + k) * 128 + tid]) * NEG_LOG2E;
      hi[k] = (float)(__bf16)v;
      lo[k] = v - hi[k];
    }
    u32x4 o = {pack2(lo[0], lo[1]), pack2(lo[2], lo[3]), pack2(hi[0], hi[1]), pack2(hi[2], hi[3])};
    *(u32x4*)(ws + WS_W3 + tid * 16) = o;
  }
}

constexpr int CAP = 416;
constexpr int RING = 2;
constexpr int L_S = 0;
constexpr int L_LIST = L_S + (2 * CAP + 32) * 64;
constexpr int L_ZEND = L_LIST + (2 * CAP + 32) * 4;
constexpr int L_WSUM = L_ZEND;
constexpr int L_RING = L_WSUM + 64;
constexpr int L_TOTAL = L_RING + 8 * RING * 4096;
constexpr unsigned OOB = 0x40000000u;

struct TileRegs {
  bf16x8 pa[4], sa[2], st[2];
};

template <bool TAIL>
DI void tile_compute(const TileRegs& t, const bf16x8 (&id)[2], const bf16x8 (&qf)[2][2], f32x16 (&agg)[2], int m) {
#pragma unroll
  for (int f = 0; f < 2; ++f) {
    f32x16 y = {};
    y = mfma(t.pa[2 * f], id[0], y);
    y = mfma(t.pa[2 * f + 1], id[1], y);
    y = mfma(t.sa[0], qf[f][0], y);
    y = mfma(t.sa[1], qf[f][1], y);
    u32x4 mk[2];
#pragma unroll
    for (int q = 0; q < 4; ++q) {
      unsigned p0 = 0, p1 = 0;
      if (!TAIL || q < m) {
        f32x2 a = {y[4 * q], y[4 * q + 1]}, b = {y[4 * q + 2], y[4 * q + 3]};
        f32x2 ea = {__builtin_amdgcn_exp2f(a[0]), __builtin_amdgcn_exp2f(a[1])};
        f32x2 eb = {__builtin_amdgcn_exp2f(b[0]), __builtin_amdgcn_exp2f(b[1])};
        ea += 1.0f;
        eb += 1.0f;
        f32x2 ra = {__builtin_amdgcn_rcpf(ea[0]), __builtin_amdgcn_rcpf(ea[1])};
        f32x2 rb = {__builtin_amdgcn_rcpf(eb[0]), __builtin_amdgcn_rcpf(eb[1])};
        a *= ra;
        b *= rb;
        p0 = pack2(a[0], a[1]);
        p1 = pack2(b[0], b[1]);
      }
      mk[q >> 1][2 * (q & 1)] = p0;
      mk[q >> 1][2 * (q & 1) + 1] = p1;
    }
    agg[f] = mfma(as_frag(mk[0]), t.st[0], agg[f]);
    agg[f] = mfma(as_frag(mk[1]), t.st[1], agg[f]);
  }
}

DI void put_record(char* lds, int sub, unsigned row, u32x3 r) {
  const unsigned rr = sub * CAP + row, sw = (rr >> 2) & 3u, k = r[0] & 255u;
  char* srow = lds + L_S + rr * 64;
  *(unsigned*)(lds + L_LIST + rr * 4) = r[0] & ~255u;
  *(unsigned short*)(srow + (((k >> 3) ^ sw) << 4) + ((k & 7u) << 1)) = 0x3F80;
  u32x4 e = {r[1], r[2], r[1], r[2]};
  *(u32x4*)(srow + ((3u ^ sw) << 4)) = e;
}

DI void clear_images(char* lds, int tid) {
  const u32x4 z = {0, 0, 0, 0}, o = {OOB, OOB, OOB, OOB};
  for (int i = tid; i < L_LIST / 16; i += 512) *(u32x4*)(lds + i * 16) = z;
  if (tid < (L_ZEND - L_LIST) / 16) *(u32x4*)(lds + L_LIST + tid * 16) = o;
}

__global__ __launch_bounds__(512, 2) void mpnn_edge(const unsigned short* __restrict__ x, const char* __restrict__ ws,
                                                    unsigned short* __restrict__ out) {
  __shared__ __attribute__((aligned(16))) char lds[L_TOTAL];
  const int tid = threadIdx.x, lane = tid & 63, w = __builtin_amdgcn_readfirstlane(tid >> 6);
  const int g = blockIdx.x, ord = (g & 7) * 32 + (g >> 3);
  const int sub = w >> 2, fh = w & 1, half = (w >> 1) & 1, ofb = w & 3;
  const int c = lane & 31, h = lane >> 5, pc = tid & 255;

  const unsigned tb = *(const unsigned*)(ws + WS_TAB + pc * 1024 + ord * 4);
  const int node = (sub * SUBN + min(c, SUBN - 1)) * 256 + g;
  const u32x4* xr = (const u32x4*)(x + (size_t)min(node, NN - 1) * 128) + h;
  const u32x4* wf = (const u32x4*)(ws + WS_WF) + lane;
  u32x4 xf[8], w2[2][8], wu[8];
#pragma unroll
  for (int s = 0; s < 8; ++s) xf[s] = xr[2 * s];
#pragma unroll
  for (int f = 0; f < 2; ++f)
#pragma unroll
    for (int s = 0; s < 8; ++s) w2[f][s] = wf[((2 * fh + f) * 8 + s) * 64];
  u32x4 w3[2];
#pragma unroll
  for (int f = 0; f < 2; ++f) w3[f] = *(const u32x4*)(ws + WS_W3 + ((2 * fh + f) * 32 + c) * 16);
  clear_images(lds, tid);

  const unsigned sA = tb & 1023u, sB = (tb >> 10) & 1023u, eB = tb >> 20;
  const unsigned first = sub ? sB : sA, cnt = sub ? eB - sB : sB - sA;
  const char* rp = ws + WS_REC + (size_t)(pc * CHUNK + first) * 12;
  u32x3 rec[4];
#pragma unroll
  for (int i = 0; i < 4; ++i) {
    rec[i] = (u32x3){0, 0, 0};
    if (i < cnt) rec[i] = *(const u32x3*)(rp + 12 * i);
  }
#pragma unroll
  for (int s = 0; s < 8; ++s) wu[s] = wf[2048 + (ofb * 8 + s) * 64];
  const unsigned incl = wave_scan(cnt);
  unsigned* wsum = (unsigned*)(lds + L_WSUM);
  if (lane == 63) wsum[w] = incl;

  bf16x8 qf[2][2];
#pragma unroll
  for (int f = 0; f < 2; ++f) {
    f32x16 q = {};
#pragma unroll
    for (int s = 0; s < 8; ++s) q = mfma(as_frag(xf[s]), as_frag(w2[f][s]), q);
    u32x4 a, b;
#pragma unroll
    for (int i = 0; i < 4; ++i) {
      a[i] = pack2(q[2 * i] * NEG_LOG2E, q[2 * i + 1] * NEG_LOG2E);
      b[i] = pack2(q[8 + 2 * i] * NEG_LOG2E, q[9 + 2 * i] * NEG_LOG2E);
      b[i] = h ? w3[f][i] : b[i];
    }
    qf[f][0] = as_frag(a);
    qf[f][1] = as_frag(b);
  }
  __syncthreads();
  const u32x4 wa = *(const u32x4*)wsum, wb = *(const u32x4*)(wsum + 4);
  const unsigned nA = wa[0] + wa[1] + wa[2] + wa[3], nB = wb[0] + wb[1] + wb[2] + wb[3];
  const u32x4 wm = sub ? wb : wa;
  const int w4 = w & 3;
  const unsigned pos = (w4 > 0 ? wm[0] : 0) + (w4 > 1 ? wm[1] : 0) + (w4 > 2 ? wm[2] : 0) + incl - cnt;
  const int ntot = __builtin_amdgcn_readfirstlane(sub ? nB : nA);
  const int nmax = __builtin_amdgcn_readfirstlane(max(nA, nB));
#pragma unroll
  for (int i = 0; i < 4; ++i)
    if (i < cnt && pos + i < CAP) put_record(lds, sub, pos + i, rec[i]);
  for (unsigned i = 4; i < cnt && pos + i < CAP; ++i) put_record(lds, sub, pos + i, *(const u32x3*)(rp + 12 * i));

  f32x16 uacc = {};
#pragma unroll
  for (int s = 0; s < 8; ++s) uacc = mfma(as_frag(wu[s]), as_frag(xf[s]), uacc);

  bf16x8 id[2];
#pragma unroll
  for (int s = 0; s < 2; ++s) {
    u32x4 v;
#pragma unroll
    for (int i = 0; i < 4; ++i) {
      const int f0 = ((2 * i) & 3) + 16 * h + 8 * (i >> 1) + 4 * s;
      v[i] = (c == f0 ? 0x3F80u : 0u) | (c == f0 + 1 ? 0x3F800000u : 0u);
    }
    id[s] = as_frag(v);
  }

  u32x4 srd;
  {
    const uint64_t pb = (uint64_t)(ws + WS_P);
    srd[0] = __builtin_amdgcn_readfirstlane((unsigned)pb);
    srd[1] = __builtin_amdgcn_readfirstlane((unsigned)(pb >> 32));
    srd[2] = P_BYTES;
    srd[3] = 0x00020000u;
  }
  const unsigned ring = (unsigned)(size_t)(LDS_AS char*)lds + L_RING + w * (RING * 4096);
  const unsigned src0 = fh * 128 + (((lane & 7) ^ (lane >> 4)) << 4), src1 = src0 ^ 64;
  const char* ringp = lds + L_RING + w * (RING * 4096) + c * 128;
  const unsigned pch = (h ^ ((c >> 1) & 7)) << 4;

  f32x16 agg[2] = {};
  for (int base = 0;;) {
    __syncthreads();
    const int nw = min(max(ntot - base, 0), CAP), oct = (nw + 7) >> 3;
    const int o0 = sub ? oct >> 1 : (oct + 1) >> 1;
    const int myo = half ? oct - o0 : o0, r0 = sub * CAP + (half ? o0 * 8 : 0);
    const int ntile = (myo + 3) >> 2;
    const char* lp = lds + L_LIST + (r0 + (lane >> 3)) * 4;
    const int sr = r0 + c;
    const char* sap = lds + L_S + sr * 64;
    const unsigned sx = (h ^ ((sr >> 2) & 3)) << 4;
    const int tq = (lane & 15) >> 2, tp = ((lane & 1) << 1) | ((lane >> 1) & 1), tch = 2 * ((lane >> 4) & 1) + (tp >> 1);
    const int tr0 = r0 + 4 * h + tq, tr1 = tr0 + 8;
    const char* stp0 = lds + L_S + tr0 * 64 + 16 * (tch ^ ((tr0 >> 2) & 3)) + 8 * (tp & 1);
    const char* stp1 = lds + L_S + tr1 * 64 + 16 * (tch ^ ((tr1 >> 2) & 3)) + 8 * (tp & 1);

    auto fetch = [&](int t) {
      const unsigned* l = (const unsigned*)(lp + t * 128);
      const unsigned v0 = l[0] + src0, v1 = l[8] + src1, v2 = l[16] + src0, v3 = l[24] + src1;
      const unsigned dstl = ring + (t % RING) * 4096;
      unsigned keep;
      asm volatile(
          "s_mov_b32 %0, m0\n\t"
          "s_mov_b32 m0, %6\n\ts_nop 0\n\tbuffer_load_dwordx4 %1, %5, 0 offen lds\n\t"
          "s_add_u32 m0, %6, 0x400\n\ts_nop 0\n\tbuffer_load_dwordx4 %2, %5, 0 offen lds\n\t"
          "s_add_u32 m0, %6, 0x800\n\ts_nop 0\n\tbuffer_load_dwordx4 %3, %5, 0 offen lds\n\t"
          "s_add_u32 m0, %6, 0xc00\n\ts_nop 0\n\tbuffer_load_dwordx4 %4, %5, 0 offen lds\n\t"
          "s_mov_b32 m0, %0"
          : "=&s"(keep)
          : "v"(v0), "v"(v1), "v"(v2), "v"(v3), "s"(srd), "s"(dstl)
          : "memory", "scc");
    };
    if (ntile > 0) fetch(0);
    if (ntile > 1) fetch(1);
    for (int t = 0; t < ntile; ++t) {
      if (t + 1 < ntile) asm volatile("s_waitcnt vmcnt(4)" ::: "memory");
      else asm volatile("s_waitcnt vmcnt(0)" ::: "memory");
      TileRegs tr;
      const char* slot = ringp + (t % RING) * 4096;
#pragma unroll
      for (int k = 0; k < 4; ++k) tr.pa[k] = as_frag(*(const u32x4*)(slot + (pch ^ (k << 5))));
      tr.sa[0] = as_frag(*(const u32x4*)(sap + t * 2048 + sx));
      tr.sa[1] = as_frag(*(const u32x4*)(sap + t * 2048 + (sx ^ 32)));
      tr.st[0] = tr_frag(stp0 + t * 2048, stp1 + t * 2048);
      tr.st[1] = tr_frag(stp0 + t * 2048 + 1024, stp1 + t * 2048 + 1024);
      asm volatile("s_waitcnt lgkmcnt(0)" : "+v"(tr.pa[0]), "+v"(tr.pa[1]), "+v"(tr.pa[2]), "+v"(tr.pa[3])::"memory");
      if (t + RING < ntile) fetch(t + RING);
      const int m = myo - 4 * t;
      if (m >= 4) tile_compute<false>(tr, id, qf, agg, 4);
      else tile_compute<true>(tr, id, qf, agg, m);
    }
    base += CAP;
    if (base >= nmax) break;
    __syncthreads();
    clear_images(lds, tid);
    __syncthreads();
    for (unsigned i = pos < (unsigned)base ? base - pos : 0; i < cnt && pos + i < (unsigned)(base + CAP); ++i)
      put_record(lds, sub, pos + i - base, *(const u32x3*)(rp + 12 * i));
  }

#pragma unroll
  for (int s = 0; s < 8; ++s) wu[s] = wf[4096 + (ofb * 8 + s) * 64];
  char* xch = lds + L_RING;
  const f32x16 give = half ? agg[0] : agg[1], mine = half ? agg[1] : agg[0];
#pragma unroll
  for (int q = 0; q < 4; ++q)
    *(f32x4*)(xch + w * (RING * 4096) + q * 1024 + lane * 16) =
        (f32x4){give[4 * q], give[4 * q + 1], give[4 * q + 2], give[4 * q + 3]};
  __syncthreads();
  u32x4 af[2];
#pragma unroll
  for (int q = 0; q < 4; ++q) {
    const f32x4 o = *(const f32x4*)(xch + (w ^ 2) * (RING * 4096) + q * 1024 + lane * 16);
    af[q >> 1][2 * (q & 1)] = pack2((mine[4 * q] + o[0]) * NEG_LN2, (mine[4 * q + 1] + o[1]) * NEG_LN2);
    af[q >> 1][2 * (q & 1) + 1] = pack2((mine[4 * q + 2] + o[2]) * NEG_LN2, (mine[4 * q + 3] + o[3]) * NEG_LN2);
  }
  char* frg = lds + L_S + sub * 8192 + lane * 16;
  *(u32x4*)(frg + (2 * (2 * fh + half)) * 1024) = af[0];
  *(u32x4*)(frg + (2 * (2 * fh + half) + 1) * 1024) = af[1];
  __syncthreads();
#pragma unroll
  for (int s = 0; s < 8; ++s) uacc = mfma(as_frag(wu[s]), as_frag(*(const u32x4*)(frg + s * 1024)), uacc);
  const int on = (sub * SUBN + c) * 256 + g;
  if (c < SUBN && on < NN) {
    unsigned short* op = out + (size_t)on * 128 + ofb * 32 + 4 * h;
#pragma unroll
    for (int q = 0; q < 4; ++q) {
      float v[4];
#pragma unroll
      for (int i = 0; i < 4; ++i) {
        const float z = uacc[4 * q + i];
        v[i] = z * __builtin_amdgcn_rcpf(1.0f + __builtin_amdgcn_exp2f(z * NEG_LOG2E));
      }
      *(u32x2*)(op + 8 * q) = (u32x2){pack2(v[0], v[1]), pack2(v[2], v[3])};
    }
  }
}

}

extern "C" void kernel_launch(void* const* d_in, const int* in_sizes, int n_in, void* d_out, int out_size, void* d_ws,
                              size_t ws_size, hipStream_t stream) {
  const unsigned short* x = (const unsigned short*)d_in[0];
  mpnn_prep<<<K1_GRID, 256, 0, stream>>>(x, (const u32x2*)d_in[1], (const int*)d_in[2], (const int*)d_in[3],
                                         (const unsigned short*)d_in[4], (const unsigned short*)d_in[5], (char*)d_ws);
  mpnn_edge<<<256, 512, 0, stream>>>(x, (const char*)d_ws, (unsigned short*)d_out);
}
```

```cpp
#include <hip/hip_runtime.h>
#include <hip/hip_bf16.h>
#include <stdint.h>

namespace {

typedef __attribute__((ext_vector_type(8))) __bf16 bf16x8;
typedef __attribute__((ext_vector_type(4))) __bf16 bf16x4;
typedef __attribute__((ext_vector_type(2))) __bf16 bf16x2;
typedef __attribute__((ext_vector_type(4))) short s16x4;
typedef __attribute__((ext_vector_type(16))) float f32x16;
typedef __attribute__((ext_vector_type(4))) float f32x4;
typedef __attribute__((ext_vector_type(2))) float f32x2;
typedef __attribute__((ext_vector_type(4))) unsigned u32x4;
typedef __attribute__((ext_vector_type(3))) unsigned u32x3;
typedef __attribute__((ext_vector_type(2))) unsigned u32x2;
#define LDS_AS __attribute__((address_space(3)))
#define DI __device__ __forceinline__

constexpr int NN = 10000, NE = 160000;
constexpr int NPROD = 256, CHUNK = 625;
constexpr int PTILES = (NN + 31) / 32;
constexpr int SUBN = 20;
constexpr float NEG_LOG2E = -1.4426950408889634f, NEG_LN2 = -0.6931471805599453f;

constexpr size_t WS_P = 0;
constexpr unsigned P_BYTES = PTILES * 32 * 256;
constexpr size_t WS_REC = WS_P + P_BYTES;
constexpr size_t WS_TAB = WS_REC + (size_t)NE * 12;
constexpr size_t WS_WF = WS_TAB + NPROD * 256 * 4;
constexpr size_t WS_W3 = WS_WF + 3 * 32768;

#define STAMP(k, i)
#define STAMPW(k, i)

constexpr int K1_SORT0 = PTILES, K1_WT0 = K1_SORT0 + NPROD, K1_GRID = K1_WT0 + 25;

DI f32x16 mfma(bf16x8 a, bf16x8 b, f32x16 c) { return __builtin_amdgcn_mfma_f32_32x32x16_bf16(a, b, c, 0, 0, 0); }
DI unsigned pack2(float a, float b) {
  bf16x2 v = {(__bf16)a, (__bf16)b};
  return __builtin_bit_cast(unsigned, v);
}
DI bf16x8 as_frag(u32x4 v) { return __builtin_bit_cast(bf16x8, v); }
DI float bf2f(unsigned short u) { return __uint_as_float((unsigned)u << 16); }
DI unsigned swap23(unsigned v) { return (v & ~12u) | ((v & 4u) << 1) | ((v & 8u) >> 1); }
DI unsigned wave_scan(unsigned v) {
  v += __builtin_amdgcn_update_dpp(0u, v, 0x111, 0xf, 0xf, false);
  v += __builtin_amdgcn_update_dpp(0u, v, 0x112, 0xf, 0xf, false);
  v += __builtin_amdgcn_update_dpp(0u, v, 0x114, 0xf, 0xf, false);
  v += __builtin_amdgcn_update_dpp(0u, v, 0x118, 0xf, 0xf, false);
  v += __builtin_amdgcn_update_dpp(0u, v, 0x142, 0xa, 0xf, false);
  v += __builtin_amdgcn_update_dpp(0u, v, 0x143, 0xc, 0xf, false);
  return v;
}
DI bf16x8 tr_frag(const char* lo, const char* hi) {
  s16x4 a = __builtin_amdgcn_ds_read_tr16_b64_v4i16((LDS_AS s16x4*)lo);
  s16x4 b = __builtin_amdgcn_ds_read_tr16_b64_v4i16((LDS_AS s16x4*)hi);
  return __builtin_bit_cast(bf16x8, __builtin_shufflevector(a, b, 0, 1, 2, 3, 4, 5, 6, 7));
}

__global__ __launch_bounds__(256) void mpnn_prep(const unsigned short* __restrict__ x, const u32x2* __restrict__ eattr,
                                                 const int* __restrict__ src, const int* __restrict__ dst,
                                                 const unsigned short* __restrict__ wmsg,
                                                 const unsigned short* __restrict__ wupd, char* __restrict__ ws) {
  __shared__ __attribute__((aligned(16))) char lds[32768];
  const int tid = threadIdx.x, lane = tid & 63, w = __builtin_amdgcn_readfirstlane(tid >> 6);
  const int b = blockIdx.x;
  STAMP(0, 0);
  if (b < K1_SORT0) {
    char* img = lds + w * 8192;
#pragma unroll
    for (int i = 0; i < 8; ++i) {
      const int row = 16 * i + (lane >> 2), ch = (lane & 3) ^ ((lane >> 4) & 3);
      __builtin_amdgcn_global_load_lds((const unsigned*)(wmsg + row * 128 + w * 32 + ch * 8),
                                       (LDS_AS unsigned*)(img + i * 1024), 16, 0, 0);
    }
    const int c = lane & 31, h = lane >> 5, node = b * 32 + c;
    const u32x4* xr = (const u32x4*)(x + (size_t)min(node, NN - 1) * 128) + h;
    u32x4 xf[8];
#pragma unroll
    for (int s = 0; s < 8; ++s) xf[s] = xr[2 * s];
    asm volatile("s_waitcnt vmcnt(0)" ::: "memory");
    STAMP(0, 1);
    const int blk = (lane >> 4) & 1, q = (lane & 15) >> 2, p = lane & 3;
    f32x16 acc = {};
#pragma unroll
    for (int s = 0; s < 8; ++s) {
      const int r0 = 16 * s + 8 * h + q, r1 = r0 + 4, ch = 2 * blk + (p >> 1);
      const bf16x8 a = tr_frag(img + 64 * r0 + 16 * (ch ^ ((r0 >> 2) & 3)) + 8 * (p & 1),
                               img + 64 * r1 + 16 * (ch ^ ((r1 >> 2) & 3)) + 8 * (p & 1));
      acc = mfma(a, as_frag(xf[s]), acc);
    }
    u32x4 o0, o1;
#pragma unroll
    for (int i = 0; i < 4; ++i) {
      o0[i] = pack2(acc[2 * i] * NEG_LOG2E, acc[2 * i + 1] * NEG_LOG2E);
      o1[i] = pack2(acc[8 + 2 * i] * NEG_LOG2E, acc[9 + 2 * i] * NEG_LOG2E);
    }
    u32x4* po = (u32x4*)(ws + WS_P + (size_t)node * 256 + w * 64 + h * 32);
    po[0] = o0;
    po[1] = o1;
    STAMP(0, 2);
    STAMPW(0, 3);
  } else if (b < K1_WT0) {
    const int pc = b - K1_SORT0, e0 = pc * CHUNK;
    unsigned* hist = (unsigned*)lds;
    unsigned* start = hist + 512 + w * 512;
    int dv[3], sv[3];
    u32x2 ev[3];
#pragma unroll
    for (int i = 0; i < 3; ++i) {
      const int k = min(tid + 256 * i, CHUNK - 1);
      dv[i] = dst[e0 + k];
      sv[i] = src[e0 + k];
      ev[i] = eattr[e0 + k];
    }
    hist[tid] = 0;
    hist[tid + 256] = 0;
    __syncthreads();
    STAMPW(0, 4);
    unsigned key[3], rank[3], col[3];
#pragma unroll
    for (int i = 0; i < 3; ++i) {
      const unsigned d = dv[i], g = d & 255u, j = d >> 8, sub = j >= SUBN;
      key[i] = (((g & 7u) << 5) + (g >> 3)) * 2 + sub;
      col[i] = swap23(j - SUBN * sub);
      rank[i] = 0;
      if (tid + 256 * i < CHUNK) rank[i] = atomicAdd(&hist[key[i]], 1u);
    }
    __syncthreads();
    STAMP(0, 5);
    const u32x4 c0 = *(const u32x4*)(hist + 8 * lane), c1 = *(const u32x4*)(hist + 8 * lane + 4);
    const unsigned tot = c0[0] + c0[1] + c0[2] + c0[3] + c1[0] + c1[1] + c1[2] + c1[3];
    u32x4 s0, s1;
    s0[0] = wave_scan(tot) - tot;
    s0[1] = s0[0] + c0[0];
    s0[2] = s0[1] + c0[1];
    s0[3] = s0[2] + c0[2];
    s1[0] = s0[3] + c0[3];
    s1[1] = s1[0] + c1[0];
    s1[2] = s1[1] + c1[1];
    s1[3] = s1[2] + c1[2];
    *(u32x4*)(start + 8 * lane) = s0;
    *(u32x4*)(start + 8 * lane + 4) = s1;
    if (w == 0) {
      u32x4 t;
      t[0] = s0[0] | (s0[1] << 10) | (s0[2] << 20);
      t[1] = s0[2] | (s0[3] << 10) | (s1[0] << 20);
      t[2] = s1[0] | (s1[1] << 10) | (s1[2] << 20);
      t[3] = s1[2] | (s1[3] << 10) | ((s1[3] + c1[3]) << 20);
      *(u32x4*)(ws + WS_TAB + pc * 1024 + lane * 16) = t;
    }
    __builtin_amdgcn_wave_barrier();
#pragma unroll
    for (int i = 0; i < 3; ++i) {
      if (tid + 256 * i < CHUNK) {
        const unsigned pos = start[key[i]] + rank[i];
        u32x3 r = {((unsigned)sv[i] << 8) | col[i], ev[i][0], ev[i][1]};
        *(u32x3*)(ws + WS_REC + (size_t)(e0 + pos) * 12) = r;
      }
    }
    STAMP(0, 6);
    STAMPW(0, 7);
  } else if (b < K1_GRID - 1) {
    const int t = (b - K1_WT0) * 256 + tid, mat = t >> 11, fb = (t >> 9) & 3, s = (t >> 6) & 7;
    const int c = lane & 31, h = lane >> 5;
    const unsigned short* wp = (mat == 0 ? wmsg + 128 * 128 : wupd + (mat - 1) * 128 * 128) + fb * 32 + c;
    unsigned v[8];
#pragma unroll
    for (int j = 0; j < 8; ++j) {
      const int k = mat == 2 ? 16 * s + 8 * (j >> 2) + 4 * h + (j & 3) : 16 * s + 8 * h + j;
      v[j] = wp[k * 128];
    }
    u32x4 o = {v[0] | (v[1] << 16), v[2] | (v[3] << 16), v[4] | (v[5] << 16), v[6] | (v[7] << 16)};
    *(u32x4*)(ws + WS_WF + (size_t)t * 16) = o;
    STAMPW(0, 8);
  } else if (tid < 128) {
    float hi[4], lo[4];
#pragma unroll
    for (int k = 0; k < 4; ++k) {
      const float v = bf2f(wmsg[(256 + k) * 128 + tid]) * NEG_LOG2E;
      hi[k] = (float)(__bf16)v;
      lo[k] = v - hi[k];
    }
    u32x4 o = {pack2(lo[0], lo[1]), pack2(lo[2], lo[3]), pack2(hi[0], hi[1]), pack2(hi[2], hi[3])};
    *(u32x4*)(ws + WS_W3 + tid * 16) = o;
  }
}

constexpr int CAP = 416;
constexpr int RING = 2;
constexpr int L_S = 0;
constexpr int L_LIST = L_S + (2 * CAP + 32) * 64;
constexpr int L_ZEND = L_LIST + (2 * CAP + 32) * 4;
constexpr int L_WSUM = L_ZEND;
constexpr int L_RING = L_WSUM + 64;
constexpr int L_TOTAL = L_RING + 8 * RING * 4096;
constexpr unsigned OOB = 0x40000000u;

struct TileRegs {
  bf16x8 pa[4], sa[2], st[2];
};

template <bool TAIL>
DI void tile_compute(const TileRegs& t, const bf16x8 (&id)[2], const bf16x8 (&qf)[2][2], f32x16 (&agg)[2], int m) {
#pragma unroll
  for (int f = 0; f < 2; ++f) {
    f32x16 y = {};
    y = mfma(t.pa[2 * f], id[0], y);
    y = mfma(t.pa[2 * f + 1], id[1], y);
    y = mfma(t.sa[0], qf[f][0], y);
    y = mfma(t.sa[1], qf[f][1], y);
    u32x4 mk[2];
#pragma unroll
    for (int q = 0; q < 4; ++q) {
      unsigned p0 = 0, p1 = 0;
      if (!TAIL || q < m) {
        f32x2 a = {y[4 * q], y[4 * q + 1]}, b = {y[4 * q + 2], y[4 * q + 3]};
        f32x2 ea = {__builtin_amdgcn_exp2f(a[0]), __builtin_amdgcn_exp2f(a[1])};
        f32x2 eb = {__builtin_amdgcn_exp2f(b[0]), __builtin_amdgcn_exp2f(b[1])};
        ea += 1.0f;
        eb += 1.0f;
        f32x2 ra = {__builtin_amdgcn_rcpf(ea[0]), __builtin_amdgcn_rcpf(ea[1])};
        f32x2 rb = {__builtin_amdgcn_rcpf(eb[0]), __builtin_amdgcn_rcpf(eb[1])};
        a *= ra;
        b *= rb;
        p0 = pack2(a[0], a[1]);
        p1 = pack2(b[0], b[1]);
      }
      mk[q >> 1][2 * (q & 1)] = p0;
      mk[q >> 1][2 * (q & 1) + 1] = p1;
    }
    agg[f] = mfma(as_frag(mk[0]), t.st[0], agg[f]);
    agg[f] = mfma(as_frag(mk[1]), t.st[1], agg[f]);
  }
}

DI void put_record(char* lds, int sub, unsigned row, u32x3 r) {
  const unsigned rr = sub * CAP + row, sw = (rr >> 2) & 3u, k = r[0] & 255u;
  char* srow = lds + L_S + rr * 64;
  *(unsigned*)(lds + L_LIST + rr * 4) = r[0] & ~255u;
  *(unsigned short*)(srow + (((k >> 3) ^ sw) << 4) + ((k & 7u) << 1)) = 0x3F80;
  u32x4 e = {r[1], r[2], r[1], r[2]};
  *(u32x4*)(srow + ((3u ^ sw) << 4)) = e;
}

DI void clear_images(char* lds, int tid) {
  const u32x4 z = {0, 0, 0, 0}, o = {OOB, OOB, OOB, OOB};
  for (int i = tid; i < L_LIST / 16; i += 512) *(u32x4*)(lds + i * 16) = z;
  if (tid < (L_ZEND - L_LIST) / 16) *(u32x4*)(lds + L_LIST + tid * 16) = o;
}

__global__ __launch_bounds__(512, 2) void mpnn_edge(const unsigned short* __restrict__ x, const char* __restrict__ ws,
                                                    unsigned short* __restrict__ out) {
  __shared__ __attribute__((aligned(16))) char lds[L_TOTAL];
  const int tid = threadIdx.x, lane = tid & 63, w = __builtin_amdgcn_readfirstlane(tid >> 6);
  const int g = blockIdx.x, ord = (g & 7) * 32 + (g >> 3);
  const int sub = w >> 2, fh = w & 1, half = (w >> 1) & 1, ofb = w & 3;
  const int c = lane & 31, h = lane >> 5, pc = tid & 255;

  STAMP(1, 0);
  const unsigned tb = *(const unsigned*)(ws + WS_TAB + pc * 1024 + ord * 4);
  const int node = (sub * SUBN + min(c, SUBN - 1)) * 256 + g;
  const u32x4* xr = (const u32x4*)(x + (size_t)min(node, NN - 1) * 128) + h;
  const u32x4* wf = (const u32x4*)(ws + WS_WF) + lane;
  u32x4 xf[8], w2[2][8], wu[8];
#pragma unroll
  for (int s = 0; s < 8; ++s) xf[s] = xr[2 * s];
#pragma unroll
  for (int f = 0; f < 2; ++f)
#pragma unroll
    for (int s = 0; s < 8; ++s) w2[f][s] = wf[((2 * fh + f) * 8 + s) * 64];
  u32x4 w3[2];
#pragma unroll
  for (int f = 0; f < 2; ++f) w3[f] = *(const u32x4*)(ws + WS_W3 + ((2 * fh + f) * 32 + c) * 16);
  clear_images(lds, tid);

  STAMP(1, 1);
  const unsigned sA = tb & 1023u, sB = (tb >> 10) & 1023u, eB = tb >> 20;
  const unsigned first = sub ? sB : sA, cnt = sub ? eB - sB : sB - sA;
  const char* rp = ws + WS_REC + (size_t)(pc * CHUNK + first) * 12;
  u32x3 rec[4];
#pragma unroll
  for (int i = 0; i < 4; ++i) {
    rec[i] = (u32x3){0, 0, 0};
    if (i < cnt) rec[i] = *(const u32x3*)(rp + 12 * i);
  }
#pragma unroll
  for (int s = 0; s < 8; ++s) wu[s] = wf[2048 + (ofb * 8 + s) * 64];
  const unsigned incl = wave_scan(cnt);
  unsigned* wsum = (unsigned*)(lds + L_WSUM);
  if (lane == 63) wsum[w] = incl;

  STAMP(1, 2);
  bf16x8 qf[2][2];
#pragma unroll
  for (int f = 0; f < 2; ++f) {
    f32x16 q = {};
#pragma unroll
    for (int s = 0; s < 8; ++s) q = mfma(as_frag(xf[s]), as_frag(w2[f][s]), q);
    u32x4 a, b;
#pragma unroll
    for (int i = 0; i < 4; ++i) {
      a[i] = pack2(q[2 * i] * NEG_LOG2E, q[2 * i + 1] * NEG_LOG2E);
      b[i] = pack2(q[8 + 2 * i] * NEG_LOG2E, q[9 + 2 * i] * NEG_LOG2E);
      b[i] = h ? w3[f][i] : b[i];
    }
    qf[f][0] = as_frag(a);
    qf[f][1] = as_frag(b);
  }
  STAMP(1, 3);
  __syncthreads();
  STAMP(1, 4);
  const u32x4 wa = *(const u32x4*)wsum, wb = *(const u32x4*)(wsum + 4);
  const unsigned nA = wa[0] + wa[1] + wa[2] + wa[3], nB = wb[0] + wb[1] + wb[2] + wb[3];
  const u32x4 wm = sub ? wb : wa;
  const int w4 = w & 3;
  const unsigned pos = (w4 > 0 ? wm[0] : 0) + (w4 > 1 ? wm[1] : 0) + (w4 > 2 ? wm[2] : 0) + incl - cnt;
  const int ntot = __builtin_amdgcn_readfirstlane(sub ? nB : nA);
  const int nmax = __builtin_amdgcn_readfirstlane(max(nA, nB));
#pragma unroll
  for (int i = 0; i < 4; ++i)
    if (i < cnt && pos + i < CAP) put_record(lds, sub, pos + i, rec[i]);
  for (unsigned i = 4; i < cnt && pos + i < CAP; ++i) put_record(lds, sub, pos + i, *(const u32x3*)(rp + 12 * i));

  f32x16 uacc = {};
#pragma unroll
  for (int s = 0; s < 8; ++s) uacc = mfma(as_frag(wu[s]), as_frag(xf[s]), uacc);

  bf16x8 id[2];
#pragma unroll
  for (int s = 0; s < 2; ++s) {
    u32x4 v;
#pragma unroll
    for (int i = 0; i < 4; ++i) {
      const int f0 = ((2 * i) & 3) + 16 * h + 8 * (i >> 1) + 4 * s;
      v[i] = (c == f0 ? 0x3F80u : 0u) | (c == f0 + 1 ? 0x3F800000u : 0u);
    }
    id[s] = as_frag(v);
  }

  u32x4 srd;
  {
    const uint64_t pb = (uint64_t)(ws + WS_P);
    srd[0] = __builtin_amdgcn_readfirstlane((unsigned)pb);
    srd[1] = __builtin_amdgcn_readfirstlane((unsigned)(pb >> 32));
    srd[2] = P_BYTES;
    srd[3] = 0x00020000u;
  }
  const unsigned ring = (unsigned)(size_t)(LDS_AS char*)lds + L_RING + w * (RING * 4096);
  const unsigned src0 = fh * 128 + (((lane & 7) ^ (lane >> 4)) << 4), src1 = src0 ^ 64;
  const char* ringp = lds + L_RING + w * (RING * 4096) + c * 128;
  const unsigned pch = (h ^ ((c >> 1) & 7)) << 4;

  f32x16 agg[2] = {};
  STAMP(1, 5);
  for (int base = 0;;) {
    __syncthreads();
    STAMP(1, 6);
    const int nw = min(max(ntot - base, 0), CAP), oct = (nw + 7) >> 3;
    const int o0 = sub ? oct >> 1 : (oct + 1) >> 1;
    const int myo = half ? oct - o0 : o0, r0 = sub * CAP + (half ? o0 * 8 : 0);
    const int ntile = (myo + 3) >> 2;
    const char* lp = lds + L_LIST + (r0 + (lane >> 3)) * 4;
    const int sr = r0 + c;
    const char* sap = lds + L_S + sr * 64;
    const unsigned sx = (h ^ ((sr >> 2) & 3)) << 4;
    const int tq = (lane & 15) >> 2, tp = ((lane & 1) << 1) | ((lane >> 1) & 1), tch = 2 * ((lane >> 4) & 1) + (tp >> 1);
    const int tr0 = r0 + 4 * h + tq, tr1 = tr0 + 8;
    const char* stp0 = lds + L_S + tr0 * 64 + 16 * (tch ^ ((tr0 >> 2) & 3)) + 8 * (tp & 1);
    const char* stp1 = lds + L_S + tr1 * 64 + 16 * (tch ^ ((tr1 >> 2) & 3)) + 8 * (tp & 1);

    auto fetch = [&](int t) {
      const unsigned* l = (const unsigned*)(lp + t * 128);
      const unsigned v0 = l[0] + src0, v1 = l[8] + src1, v2 = l[16] + src0, v3 = l[24] + src1;
      const unsigned dstl = ring + (t % RING) * 4096;
      unsigned keep;
      asm volatile(
          "s_mov_b32 %0, m0\n\t"
          "s_mov_b32 m0, %6\n\ts_nop 0\n\tbuffer_load_dwordx4 %1, %5, 0 offen lds\n\t"
          "s_add_u32 m0, %6, 0x400\n\ts_nop 0\n\tbuffer_load_dwordx4 %2, %5, 0 offen lds\n\t"
          "s_add_u32 m0, %6, 0x800\n\ts_nop 0\n\tbuffer_load_dwordx4 %3, %5, 0 offen lds\n\t"
          "s_add_u32 m0, %6, 0xc00\n\ts_nop 0\n\tbuffer_load_dwordx4 %4, %5, 0 offen lds\n\t"
          "s_mov_b32 m0, %0"
          : "=&s"(keep)
          : "v"(v0), "v"(v1), "v"(v2), "v"(v3), "s"(srd), "s"(dstl)
          : "memory", "scc");
    };
    if (ntile > 0) fetch(0);
    if (ntile > 1) fetch(1);
    for (int t = 0; t < ntile; ++t) {
      if (t + 1 < ntile) asm volatile("s_waitcnt vmcnt(4)" ::: "memory");
      else asm volatile("s_waitcnt vmcnt(0)" ::: "memory");
      if (t == 0) STAMP(1, 7);
      TileRegs tr;
      const char* slot = ringp + (t % RING) * 4096;
#pragma unroll
      for (int k = 0; k < 4; ++k) tr.pa[k] = as_frag(*(const u32x4*)(slot + (pch ^ (k << 5))));
      tr.sa[0] = as_frag(*(const u32x4*)(sap + t * 2048 + sx));
      tr.sa[1] = as_frag(*(const u32x4*)(sap + t * 2048 + (sx ^ 32)));
      tr.st[0] = tr_frag(stp0 + t * 2048, stp1 + t * 2048);
      tr.st[1] = tr_frag(stp0 + t * 2048 + 1024, stp1 + t * 2048 + 1024);
      asm volatile("s_waitcnt lgkmcnt(0)" : "+v"(tr.pa[0]), "+v"(tr.pa[1]), "+v"(tr.pa[2]), "+v"(tr.pa[3])::"memory");
      if (t + RING < ntile) fetch(t + RING);
      const int m = myo - 4 * t;
      if (m >= 4) tile_compute<false>(tr, id, qf, agg, 4);
      else tile_compute<true>(tr, id, qf, agg, m);
    }
    base += CAP;
    if (base >= nmax) break;
    __syncthreads();
    clear_images(lds, tid);
    __syncthreads();
    for (unsigned i = pos < (unsigned)base ? base - pos : 0; i < cnt && pos + i < (unsigned)(base + CAP); ++i)
      put_record(lds, sub, pos + i - base, *(const u32x3*)(rp + 12 * i));
  }

  STAMP(1, 8);
#pragma unroll
  for (int s = 0; s < 8; ++s) wu[s] = wf[4096 + (ofb * 8 + s) * 64];
  char* xch = lds + L_RING;
  const f32x16 give = half ? agg[0] : agg[1], mine = half ? agg[1] : agg[0];
#pragma unroll
  for (int q = 0; q < 4; ++q)
    *(f32x4*)(xch + w * (RING * 4096) + q * 1024 + lane * 16) =
        (f32x4){give[4 * q], give[4 * q + 1], give[4 * q + 2], give[4 * q + 3]};
  __syncthreads();
  u32x4 af[2];
#pragma unroll
  for (int q = 0; q < 4; ++q) {
    const f32x4 o = *(const f32x4*)(xch + (w ^ 2) * (RING * 4096) + q * 1024 + lane * 16);
    af[q >> 1][2 * (q & 1)] = pack2((mine[4 * q] + o[0]) * NEG_LN2, (mine[4 * q + 1] + o[1]) * NEG_LN2);
    af[q >> 1][2 * (q & 1) + 1] = pack2((mine[4 * q + 2] + o[2]) * NEG_LN2, (mine[4 * q + 3] + o[3]) * NEG_LN2);
  }
  char* frg = lds + L_S + sub * 8192 + lane * 16;
  *(u32x4*)(frg + (2 * (2 * fh + half)) * 1024) = af[0];
  *(u32x4*)(frg + (2 * (2 * fh + half) + 1) * 1024) = af[1];
  __syncthreads();
#pragma unroll
  for (int s = 0; s < 8; ++s) uacc = mfma(as_frag(wu[s]), as_frag(*(const u32x4*)(frg + s * 1024)), uacc);
  STAMP(1, 9);
  const int on = (sub * SUBN + c) * 256 + g;
  if (c < SUBN && on < NN) {
    unsigned short* op = out + (size_t)on * 128 + ofb * 32 + 4 * h;
#pragma unroll
    for (int q = 0; q < 4; ++q) {
      float v[4];
#pragma unroll
      for (int i = 0; i < 4; ++i) {
        const float z = uacc[4 * q + i];
        v[i] = z * __builtin_amdgcn_rcpf(1.0f + __builtin_amdgcn_exp2f(z * NEG_LOG2E));
      }
      *(u32x2*)(op + 8 * q) = (u32x2){pack2(v[0], v[1]), pack2(v[2], v[3])};
    }
  }
  STAMP(1, 10);
  STAMPW(1, 11);
}

}

extern "C" void kernel_launch(void* const* d_in, const int* in_sizes, int n_in, void* d_out, int out_size, void* d_ws,
                              size_t ws_size, hipStream_t stream) {
  const unsigned short* x = (const unsigned short*)d_in[0];
  mpnn_prep<<<K1_GRID, 256, 0, stream>>>(x, (const u32x2*)d_in[1], (const int*)d_in[2], (const int*)d_in[3],
                                         (const unsigned short*)d_in[4], (const unsigned short*)d_in[5], (char*)d_ws);
  mpnn_edge<<<256, 512, 0, stream>>>(x, (const char*)d_ws, (unsigned short*)d_out);
}
```

```cpp
#include <hip/hip_runtime.h>
#include <hip/hip_bf16.h>
#include <stdint.h>

namespace {

typedef __attribute__((ext_vector_type(8))) __bf16 bf16x8;
typedef __attribute__((ext_vector_type(2))) __bf16 bf16x2;
typedef __attribute__((ext_vector_type(4))) short s16x4;
typedef __attribute__((ext_vector_type(16))) float f32x16;
typedef __attribute__((ext_vector_type(4))) float f32x4;
typedef __attribute__((ext_vector_type(2))) float f32x2;
typedef __attribute__((ext_vector_type(16))) unsigned u32x16;
typedef __attribute__((ext_vector_type(4))) unsigned u32x4;
typedef __attribute__((ext_vector_type(2))) unsigned u32x2;
struct __attribute__((packed, aligned(4))) Rec {
  unsigned a, e01, e23;
};
#define LDS_AS __attribute__((address_space(3)))
#define DI __device__ __forceinline__

constexpr int NN = 10000, NE = 160000;
constexpr int NPROD = 64, CHUNK = NE / NPROD;
constexpr int PBLK = (NN + 63) / 64;
constexpr int SUBN = 20;
constexpr float NEG_LOG2E = -1.4426950408889634f;

constexpr size_t WS_P = 0;
constexpr unsigned P_BYTES = PBLK * 64 * 256;
constexpr size_t WS_REC = WS_P + P_BYTES;
constexpr size_t WS_TAB = WS_REC + (size_t)NE * 12;
constexpr size_t WS_W2 = WS_TAB + NPROD * 2048;
constexpr size_t WS_WU = WS_W2 + 32768;
constexpr size_t WS_W3 = WS_WU + 65536;

#define STAMP_DECL
#define STAMP(i)
#define STAMPW(i)
#define STAMP_OUT(k, wv)

constexpr int K1_P0 = NPROD, K1_WT0 = K1_P0 + PBLK, K1_GRID = K1_WT0 + 13;

DI f32x16 mfma32(bf16x8 a, bf16x8 b, f32x16 c) { return __builtin_amdgcn_mfma_f32_32x32x16_bf16(a, b, c, 0, 0, 0); }
DI f32x4 mfma16(bf16x8 a, bf16x8 b, f32x4 c) { return __builtin_amdgcn_mfma_f32_16x16x32_bf16(a, b, c, 0, 0, 0); }
DI unsigned pack2(float a, float b) {
  bf16x2 v = {(__bf16)a, (__bf16)b};
  return __builtin_bit_cast(unsigned, v);
}
DI bf16x8 as_frag(u32x4 v) { return __builtin_bit_cast(bf16x8, v); }
DI bf16x8 sub_frag(const u32x16& v, int s) { return as_frag((u32x4){v[4 * s], v[4 * s + 1], v[4 * s + 2], v[4 * s + 3]}); }
DI bf16x8 lds_frag(const char* p) { return as_frag(*(const u32x4*)p); }
DI float bf2f(unsigned short u) { return __uint_as_float((unsigned)u << 16); }
DI unsigned swap23(unsigned v) { return (v & ~12u) | ((v & 4u) << 1) | ((v & 8u) >> 1); }
DI unsigned lds_addr(const char* p) { return (unsigned)(size_t)(const LDS_AS char*)p; }
DI unsigned wave_scan(unsigned v) {
  v += __builtin_amdgcn_update_dpp(0u, v, 0x111, 0xf, 0xf, false);
  v += __builtin_amdgcn_update_dpp(0u, v, 0x112, 0xf, 0xf, false);
  v += __builtin_amdgcn_update_dpp(0u, v, 0x114, 0xf, 0xf, false);
  v += __builtin_amdgcn_update_dpp(0u, v, 0x118, 0xf, 0xf, false);
  v += __builtin_amdgcn_update_dpp(0u, v, 0x142, 0xa, 0xf, false);
  v += __builtin_amdgcn_update_dpp(0u, v, 0x143, 0xc, 0xf, false);
  return v;
}
DI bf16x8 tr_frag(const char* lo, const char* hi) {
  s16x4 a = __builtin_amdgcn_ds_read_tr16_b64_v4i16((LDS_AS s16x4*)lo);
  s16x4 b = __builtin_amdgcn_ds_read_tr16_b64_v4i16((LDS_AS s16x4*)hi);
  return __builtin_bit_cast(bf16x8, __builtin_shufflevector(a, b, 0, 1, 2, 3, 4, 5, 6, 7));
}
DI u32x4 make_srd(const void* base, unsigned bytes) {
  const uint64_t pb = (uint64_t)base;
  u32x4 srd;
  srd[0] = __builtin_amdgcn_readfirstlane((unsigned)pb);
  srd[1] = __builtin_amdgcn_readfirstlane((unsigned)(pb >> 32));
  srd[2] = bytes;
  srd[3] = 0x00020000u;
  return srd;
}
DI void dma16(u32x4 srd, unsigned voff, unsigned dst) {
  unsigned keep;
  asm volatile("s_mov_b32 %0, m0\n\ts_mov_b32 m0, %3\n\ts_nop 2\n\tbuffer_load_dwordx4 %1, %2, 0 offen lds\n\ts_mov_b32 m0, %0"
               : "=&s"(keep)
               : "v"(voff), "s"(srd), "s"(dst)
               : "memory");
}
DI void barrier_lds() {
  asm volatile("s_waitcnt lgkmcnt(0)\n\ts_barrier" ::: "memory");
}
DI void barrier_all() {
  asm volatile("s_waitcnt vmcnt(0) lgkmcnt(0)\n\ts_barrier" ::: "memory");
}

constexpr int K1_LDS = 83968;

__global__ __launch_bounds__(512) void mpnn_prep(const unsigned short* __restrict__ x, const u32x2* __restrict__ eattr,
                                                 const int* __restrict__ src, const int* __restrict__ dst,
                                                 const unsigned short* __restrict__ wmsg,
                                                 const unsigned short* __restrict__ wupd, char* __restrict__ ws) {
  __shared__ __attribute__((aligned(16))) char lds[K1_LDS];
  const int tid = threadIdx.x, lane = tid & 63, w = __builtin_amdgcn_readfirstlane(tid >> 6);
  const int b = blockIdx.x;
  STAMP_DECL
  STAMP(0);
  if (b < K1_P0) {
    const int e0 = b * CHUNK;
    unsigned* hist = (unsigned*)lds;
    unsigned short* start = (unsigned short*)(lds + 2048 + w * 1024);
    unsigned* stage = (unsigned*)(lds + 10240);
    int dv[5], sv[5];
    u32x2 ev[5];
#pragma unroll
    for (int i = 0; i < 5; ++i) dv[i] = dst[e0 + min(tid + 512 * i, CHUNK - 1)];
#pragma unroll
    for (int i = 0; i < 5; ++i) {
      const int k = min(tid + 512 * i, CHUNK - 1);
      sv[i] = src[e0 + k];
      ev[i] = eattr[e0 + k];
    }
    hist[tid] = 0;
    barrier_lds();
    unsigned key[5], rank[5];
#pragma unroll
    for (int i = 0; i < 5; ++i) {
      const unsigned d = dv[i], g = d & 255u, j = d >> 8, sub = j >= SUBN;
      key[i] = (((g & 7u) << 5) + (g >> 3)) * 2 + sub;
      dv[i] = swap23(j - SUBN * sub);
      rank[i] = 0;
      if (tid + 512 * i < CHUNK) rank[i] = atomicAdd(&hist[key[i]], 1u);
    }
    STAMP(1);
    barrier_lds();
    const u32x4 c0 = *(const u32x4*)(hist + 8 * lane), c1 = *(const u32x4*)(hist + 8 * lane + 4);
    const unsigned tot = c0[0] + c0[1] + c0[2] + c0[3] + c1[0] + c1[1] + c1[2] + c1[3];
    unsigned s[9];
    s[0] = wave_scan(tot) - tot;
#pragma unroll
    for (int i = 0; i < 4; ++i) s[i + 1] = s[i] + c0[i];
#pragma unroll
    for (int i = 0; i < 4; ++i) s[i + 5] = s[i + 4] + c1[i];
    const u32x4 sp = {s[0] | (s[1] << 16), s[2] | (s[3] << 16), s[4] | (s[5] << 16), s[6] | (s[7] << 16)};
    *(u32x4*)(start + 8 * lane) = sp;
    if (w < 2) {
      const u32x4 t = w ? (u32x4){sp[2], s[6], sp[3], s[8]} : (u32x4){sp[0], s[2], sp[1], s[4]};
      *(u32x4*)(ws + WS_TAB + b * 2048 + lane * 32 + w * 16) = t;
    }
    __builtin_amdgcn_wave_barrier();
#pragma unroll
    for (int i = 0; i < 5; ++i) {
      if (tid + 512 * i < CHUNK) {
        unsigned* r = stage + 3 * (start[key[i]] + rank[i]);
        r[0] = ((unsigned)sv[i] << 8) | dv[i];
        r[1] = ev[i][0];
        r[2] = ev[i][1];
      }
    }
    STAMP(2);
    barrier_lds();
    u32x4* go = (u32x4*)(ws + WS_REC + (size_t)e0 * 12);
#pragma unroll
    for (int i = 0; i < 4; ++i) {
      const int k = tid + 512 * i;
      if (k < CHUNK * 12 / 16) go[k] = ((const u32x4*)stage)[k];
    }
    STAMP(3);
    STAMPW(4);
  } else if (b < K1_WT0) {
    const int n0 = (b - K1_P0) * 64;
    char* wimg = lds;
    char* ximg = lds + 32768;
    char* oimg = lds + 49152;
    const u32x4 xs = make_srd(x, NN * 256), wsd = make_srd(wmsg, 128 * 256);
    const unsigned r4 = lane >> 4, c16 = lane & 15;
#pragma unroll
    for (int i = 0; i < 2; ++i) {
      const unsigned row = (w * 2 + i) * 4 + r4;
      dma16(xs, (n0 + row) * 256 + ((c16 ^ (row & 15)) << 4), lds_addr(ximg) + (w * 2 + i) * 1024);
    }
#pragma unroll
    for (int i = 0; i < 4; ++i) {
      const unsigned row = (w * 4 + i) * 4 + r4;
      dma16(wsd, row * 256 + ((c16 ^ (((row & 3) << 2) | ((row >> 2) & 3))) << 4), lds_addr(wimg) + (w * 4 + i) * 1024);
    }
    barrier_all();
    STAMP(5);
    const int c = lane & 31, h = lane >> 5, fb = w & 3, xrow = (w >> 2) * 32 + c;
    const int blk = (lane >> 4) & 1, q = (lane & 15) >> 2, p = lane & 3, ch = 4 * fb + 2 * blk + (p >> 1);
    f32x16 acc = {};
#pragma unroll
    for (int s = 0; s < 8; ++s) {
      const int r0 = 16 * s + 8 * h + q, r1 = r0 + 4;
      const bf16x8 a = tr_frag(wimg + 256 * r0 + 16 * (ch ^ (((r0 & 3) << 2) | ((r0 >> 2) & 3))) + 8 * (p & 1),
                               wimg + 256 * r1 + 16 * (ch ^ (((r1 & 3) << 2) | ((r1 >> 2) & 3))) + 8 * (p & 1));
      acc = mfma32(a, lds_frag(ximg + 256 * xrow + 16 * ((2 * s + h) ^ (xrow & 15))), acc);
    }
    u32x4 o0, o1;
#pragma unroll
    for (int i = 0; i < 4; ++i) {
      o0[i] = pack2(acc[2 * i] * NEG_LOG2E, acc[2 * i + 1] * NEG_LOG2E);
      o1[i] = pack2(acc[8 + 2 * i] * NEG_LOG2E, acc[9 + 2 * i] * NEG_LOG2E);
    }
    *(u32x4*)(oimg + 256 * xrow + 16 * ((4 * fb + 2 * h) ^ (xrow & 15))) = o0;
    *(u32x4*)(oimg + 256 * xrow + 16 * ((4 * fb + 2 * h + 1) ^ (xrow & 15))) = o1;
    STAMP(6);
    barrier_lds();
#pragma unroll
    for (int i = 0; i < 2; ++i) {
      const int k = tid + 512 * i, row = k >> 4;
      *(u32x4*)(ws + WS_P + (size_t)n0 * 256 + k * 16) = *(const u32x4*)(oimg + 256 * row + 16 * ((k & 15) ^ (row & 15)));
    }
    STAMP(7);
    STAMPW(8);
  } else if (b < K1_GRID - 1) {
    const int t = (b - K1_WT0) * 512 + tid;
    const unsigned short* wp;
    if (t < 2048) wp = wmsg + (128 + 16 * ((t >> 6) & 7) + 8 * (lane >> 5)) * 128 + (t >> 9) * 32 + (lane & 31);
    else wp = wupd + (32 * ((t >> 6) & 7) + 8 * (lane >> 4)) * 128 + ((t - 2048) >> 9) * 16 + (lane & 15);
    unsigned v[8];
#pragma unroll
    for (int j = 0; j < 8; ++j) v[j] = wp[j * 128];
    u32x4 o = {v[0] | (v[1] << 16), v[2] | (v[3] << 16), v[4] | (v[5] << 16), v[6] | (v[7] << 16)};
    *(u32x4*)(ws + WS_W2 + (size_t)t * 16) = o;
    STAMPW(9);
  } else if (tid < 128) {
    float hi[4], lo[4];
#pragma unroll
    for (int k = 0; k < 4; ++k) {
      const float v = bf2f(wmsg[(256 + k) * 128 + tid]) * NEG_LOG2E;
      hi[k] = (float)(__bf16)v;
      lo[k] = v - hi[k];
    }
    u32x4 o = {pack2(lo[0], lo[1]), pack2(lo[2], lo[3]), pack2(hi[0], hi[1]), pack2(hi[2], hi[3])};
    *(u32x4*)(ws + WS_W3 + tid * 16) = o;
  }
  STAMP_OUT(0, 0);
}

constexpr int CAP = 416;
constexpr int L_S = 0;
constexpr int L_LIST = L_S + (2 * CAP + 32) * 64;
constexpr int L_ZEND = L_LIST + (2 * CAP + 32) * 4;
constexpr int L_WSUM = L_ZEND;
constexpr int L_X = L_WSUM + 128;
constexpr int L_RING = L_X + 48 * 256;
constexpr int L_TOTAL = L_RING + 8 * 8192;
constexpr int L_AGG = L_S, L_OUT = L_S + 48 * 256;
constexpr unsigned OOB = 0x40000000u;
constexpr int NREC = 5;

DI void edge_tiles(f32x16& agg0, f32x16& agg1, u32x16& wu0, u32x16& wu1, bf16x8 q00, bf16x8 q01, bf16x8 q10, bf16x8 q11,
                   bf16x8 id0, bf16x8 id1, unsigned pa0, unsigned sa0, unsigned sa1, unsigned st0, unsigned st1,
                   unsigned lst, unsigned s0, unsigned wuo, u32x4 srd, const char* wub, unsigned ring, int nf, int m) {
  int k = nf + (m > 0), t;
  unsigned keep;
  asm volatile(
      "s_waitcnt lgkmcnt(0)\n\t"
      "s_mov_b32 %[keep], m0\n\t"
      "ds_read_b32 v216, %[lst] offset:0\n\t"
      "ds_read_b32 v218, %[lst] offset:32\n\t"
      "ds_read_b32 v220, %[lst] offset:64\n\t"
      "ds_read_b32 v222, %[lst] offset:96\n\t"
      "ds_read_b32 v160, %[lst] offset:128\n\t"
      "ds_read_b32 v162, %[lst] offset:160\n\t"
      "ds_read_b32 v164, %[lst] offset:192\n\t"
      "ds_read_b32 v166, %[lst] offset:224\n\t"
      "ds_read_b32 v168, %[lst] offset:256\n\t"
      "ds_read_b32 v170, %[lst] offset:288\n\t"
      "ds_read_b32 v172, %[lst] offset:320\n\t"
      "ds_read_b32 v174, %[lst] offset:352\n\t"
      "v_mov_b32 v217, %[s0]\n\t"
      "v_mov_b32 v219, %[s1]\n\t"
      "v_mov_b32 v221, %[s0]\n\t"
      "v_mov_b32 v223, %[s1]\n\t"
      "v_mov_b32 v161, %[s0]\n\t"
      "v_mov_b32 v163, %[s1]\n\t"
      "v_mov_b32 v165, %[s0]\n\t"
      "v_mov_b32 v167, %[s1]\n\t"
      "v_mov_b32 v169, %[s0]\n\t"
      "v_mov_b32 v171, %[s1]\n\t"
      "v_mov_b32 v173, %[s0]\n\t"
      "v_mov_b32 v175, %[s1]\n\t"
      "s_waitcnt lgkmcnt(8)\n\t"
      "s_cmp_lt_i32 %[k], 1\n\t"
      "s_cbranch_scc1 Lnd1_%=\n\t"
      "s_add_u32 m0, %[ring], 0\n\t"
      "s_nop 0\n\t"
      "buffer_load_dwordx4 v[216:217], %[srd], 0 idxen offen lds\n\t"
      "s_add_u32 m0, %[ring], 1024\n\t"
      "s_nop 0\n\t"
      "buffer_load_dwordx4 v[218:219], %[srd], 0 idxen offen lds\n\t"
      "s_add_u32 m0, %[ring], 2048\n\t"
      "s_nop 0\n\t"
      "buffer_load_dwordx4 v[220:221], %[srd], 0 idxen offen lds\n\t"
      "s_add_u32 m0, %[ring], 3072\n\t"
      "s_nop 0\n\t"
      "buffer_load_dwordx4 v[222:223], %[srd], 0 idxen offen lds\n\t"
      "s_branch Lge1_%=\n\t"
      "Lnd1_%=:\n\t"
      "s_cmp_lg_i32 %[k], 0\n\t"
      "s_cbranch_scc1 Lge1_%=\n\t"
      "global_load_dwordx4 v[224:227], %[wuo], %[wub] offset:-4096\n\t"
      "global_load_dwordx4 v[228:231], %[wuo], %[wub] offset:-3072\n\t"
      "global_load_dwordx4 v[232:235], %[wuo], %[wub] offset:-2048\n\t"
      "global_load_dwordx4 v[236:239], %[wuo], %[wub] offset:-1024\n\t"
      "global_load_dwordx4 v[240:243], %[wuo], %[wub] offset:0\n\t"
      "global_load_dwordx4 v[244:247], %[wuo], %[wub] offset:1024\n\t"
      "global_load_dwordx4 v[248:251], %[wuo], %[wub] offset:2048\n\t"
      "global_load_dwordx4 v[252:255], %[wuo], %[wub] offset:3072\n\t"
      "Lge1_%=:\n\t"
      "s_sub_i32 %[k], %[k], 1\n\t"
      "s_waitcnt lgkmcnt(4)\n\t"
      "s_cmp_lt_i32 %[k], 1\n\t"
      "s_cbranch_scc1 Lnd2_%=\n\t"
      "s_add_u32 m0, %[ring], 4096\n\t"
      "s_nop 0\n\t"
      "buffer_load_dwordx4 v[160:161], %[srd], 0 idxen offen lds\n\t"
      "s_add_u32 m0, %[ring], 5120\n\t"
      "s_nop 0\n\t"
      "buffer_load_dwordx4 v[162:163], %[srd], 0 idxen offen lds\n\t"
      "s_add_u32 m0, %[ring], 6144\n\t"
      "s_nop 0\n\t"
      "buffer_load_dwordx4 v[164:165], %[srd], 0 idxen offen lds\n\t"
      "s_add_u32 m0, %[ring], 7168\n\t"
      "s_nop 0\n\t"
      "buffer_load_dwordx4 v[166:167], %[srd], 0 idxen offen lds\n\t"
      "s_branch Lge2_%=\n\t"
      "Lnd2_%=:\n\t"
      "s_cmp_lg_i32 %[k], 0\n\t"
      "s_cbranch_scc1 Lge2_%=\n\t"
      "global_load_dwordx4 v[224:227], %[wuo], %[wub] offset:-4096\n\t"
      "global_load_dwordx4 v[228:231], %[wuo], %[wub] offset:-3072\n\t"
      "global_load_dwordx4 v[232:235], %[wuo], %[wub] offset:-2048\n\t"
      "global_load_dwordx4 v[236:239], %[wuo], %[wub] offset:-1024\n\t"
      "global_load_dwordx4 v[240:243], %[wuo], %[wub] offset:0\n\t"
      "global_load_dwordx4 v[244:247], %[wuo], %[wub] offset:1024\n\t"
      "global_load_dwordx4 v[248:251], %[wuo], %[wub] offset:2048\n\t"
      "global_load_dwordx4 v[252:255], %[wuo], %[wub] offset:3072\n\t"
      "Lge2_%=:\n\t"
      "s_sub_i32 %[k], %[k], 1\n\t"
      "v_mov_b32 v176, 0\n\t"
      "v_mov_b32 v177, 0\n\t"
      "v_mov_b32 v178, 0\n\t"
      "v_mov_b32 v179, 0\n\t"
      "v_mov_b32 v180, 0\n\t"
      "v_mov_b32 v181, 0\n\t"
      "v_mov_b32 v182, 0\n\t"
      "v_mov_b32 v183, 0\n\t"
      "v_mov_b32 v208, 0\n\t"
      "v_mov_b32 v209, 0\n\t"
      "v_mov_b32 v210, 0\n\t"
      "v_mov_b32 v211, 0\n\t"
      "v_mov_b32 v212, 0\n\t"
      "v_mov_b32 v213, 0\n\t"
      "v_mov_b32 v214, 0\n\t"
      "v_mov_b32 v215, 0\n\t"
      "s_add_u32 %[t], %[nf], %[m]\n\t"
      "s_cmp_eq_u32 %[t], 0\n\t"
      "s_cbranch_scc1 Ldone_%=\n\t"
      "ds_read_b128 v[200:203], %[sa0] offset:0\n\t"
      "ds_read_b128 v[204:207], %[sa1] offset:0\n\t"
      "s_cmp_lt_i32 %[k], 0\n\t"
      "s_cbranch_scc1 Lw83_%=\n\t"
      "s_waitcnt vmcnt(4)\n\t"
      "Lw83_%=:\n\t"
      "s_waitcnt vmcnt(8)\n\t"
      "ds_read_b128 v[184:187], %[pa0] offset:0\n\t"
      "ds_read_b128 v[188:191], %[pa1] offset:0\n\t"
      "ds_read_b128 v[192:195], %[pa2] offset:0\n\t"
      "ds_read_b128 v[196:199], %[pa3] offset:0\n\t"
      "s_waitcnt lgkmcnt(4)\n\t"
      "v_mfma_f32_32x32x16_bf16 v[128:143], v[200:203], %[q00], 0\n\t"
      "v_mfma_f32_32x32x16_bf16 v[128:143], v[204:207], %[q01], v[128:143]\n\t"
      "s_waitcnt lgkmcnt(0)\n\t"
      "s_cmp_lt_i32 %[k], 1\n\t"
      "s_cbranch_scc1 Lnd4_%=\n\t"
      "s_add_u32 m0, %[ring], 0\n\t"
      "s_nop 0\n\t"
      "buffer_load_dwordx4 v[168:169], %[srd], 0 idxen offen lds\n\t"
      "s_add_u32 m0, %[ring], 1024\n\t"
      "s_nop 0\n\t"
      "buffer_load_dwordx4 v[170:171], %[srd], 0 idxen offen lds\n\t"
      "s_add_u32 m0, %[ring], 2048\n\t"
      "s_nop 0\n\t"
      "buffer_load_dwordx4 v[172:173], %[srd], 0 idxen offen lds\n\t"
      "s_add_u32 m0, %[ring], 3072\n\t"
      "s_nop 0\n\t"
      "buffer_load_dwordx4 v[174:175], %[srd], 0 idxen offen lds\n\t"
      "s_branch Lge4_%=\n\t"
      "Lnd4_%=:\n\t"
      "s_cmp_lg_i32 %[k], 0\n\t"
      "s_cbranch_scc1 Lge4_%=\n\t"
      "global_load_dwordx4 v[224:227], %[wuo], %[wub] offset:-4096\n\t"
      "global_load_dwordx4 v[228:231], %[wuo], %[wub] offset:-3072\n\t"
      "global_load_dwordx4 v[232:235], %[wuo], %[wub] offset:-2048\n\t"
      "global_load_dwordx4 v[236:239], %[wuo], %[wub] offset:-1024\n\t"
      "global_load_dwordx4 v[240:243], %[wuo], %[wub] offset:0\n\t"
      "global_load_dwordx4 v[244:247], %[wuo], %[wub] offset:1024\n\t"
      "global_load_dwordx4 v[248:251], %[wuo], %[wub] offset:2048\n\t"
      "global_load_dwordx4 v[252:255], %[wuo], %[wub] offset:3072\n\t"
      "Lge4_%=:\n\t"
      "s_sub_i32 %[k], %[k], 1\n\t"
      "v_mfma_f32_32x32x16_bf16 v[128:143], v[184:187], %[id0], v[128:143]\n\t"
      "v_mfma_f32_32x32x16_bf16 v[128:143], v[188:191], %[id1], v[128:143]\n\t"
      "s_mov_b32 %[t], %[nf]\n\t"
      "s_cmp_eq_u32 %[t], 0\n\t"
      "s_nop 9\n\t"
      "s_cbranch_scc1 Ltail_%=\n\t"
      "Lloop_%=:\n\t"
      "v_exp_f32 v160, v128\n\t"
      "v_mfma_f32_32x32x16_bf16 v[144:159], v[200:203], %[q10], 0\n\t"
      "v_exp_f32 v161, v129\n\t"
      "v_exp_f32 v162, v130\n\t"
      "v_exp_f32 v163, v131\n\t"
      "v_exp_f32 v164, v132\n\t"
      "v_mfma_f32_32x32x16_bf16 v[144:159], v[204:207], %[q11], v[144:159]\n\t"
      "v_exp_f32 v165, v133\n\t"
      "s_cmp_lt_i32 %[k], 0\n\t"
      "s_cbranch_scc1 Lw85_%=\n\t"
      "s_waitcnt vmcnt(4)\n\t"
      "Lw85_%=:\n\t"
      "s_waitcnt vmcnt(8)\n\t"
      "ds_read_b128 v[200:203], %[sa0] offset:2048\n\t"
      "ds_read_b128 v[204:207], %[sa1] offset:2048\n\t"
      "ds_read_b128 v[184:187], %[pa0] offset:4096\n\t"
      "ds_read_b128 v[188:191], %[pa1] offset:4096\n\t"
      "v_exp_f32 v166, v134\n\t"
      "v_exp_f32 v167, v135\n\t"
      "v_exp_f32 v168, v136\n\t"
      "v_mfma_f32_32x32x16_bf16 v[144:159], v[192:195], %[id0], v[144:159]\n\t"
      "v_exp_f32 v169, v137\n\t"
      "v_exp_f32 v170, v138\n\t"
      "v_exp_f32 v171, v139\n\t"
      "v_exp_f32 v172, v140\n\t"
      "v_exp_f32 v173, v141\n\t"
      "v_exp_f32 v174, v142\n\t"
      "v_exp_f32 v175, v143\n\t"
      "v_pk_add_f32 v[160:161], v[160:161], 1.0 op_sel_hi:[1,0]\n\t"
      "v_pk_add_f32 v[162:163], v[162:163], 1.0 op_sel_hi:[1,0]\n\t"
      "v_pk_add_f32 v[164:165], v[164:165], 1.0 op_sel_hi:[1,0]\n\t"
      "v_pk_add_f32 v[166:167], v[166:167], 1.0 op_sel_hi:[1,0]\n\t"
      "v_pk_add_f32 v[168:169], v[168:169], 1.0 op_sel_hi:[1,0]\n\t"
      "v_pk_add_f32 v[170:171], v[170:171], 1.0 op_sel_hi:[1,0]\n\t"
      "v_pk_add_f32 v[172:173], v[172:173], 1.0 op_sel_hi:[1,0]\n\t"
      "v_pk_add_f32 v[174:175], v[174:175], 1.0 op_sel_hi:[1,0]\n\t"
      "v_rcp_f32 v160, v160\n\t"
      "v_mfma_f32_32x32x16_bf16 v[144:159], v[196:199], %[id1], v[144:159]\n\t"
      "v_rcp_f32 v161, v161\n\t"
      "ds_read_b128 v[192:195], %[pa2] offset:4096\n\t"
      "ds_read_b128 v[196:199], %[pa3] offset:4096\n\t"
      "ds_read_b32 v216, %[lst] offset:384\n\t"
      "ds_read_b32 v218, %[lst] offset:416\n\t"
      "ds_read_b32 v220, %[lst] offset:448\n\t"
      "ds_read_b32 v222, %[lst] offset:480\n\t"
      "v_rcp_f32 v162, v162\n\t"
      "v_rcp_f32 v163, v163\n\t"
      "v_rcp_f32 v164, v164\n\t"
      "v_mfma_f32_32x32x16_bf16 %[ag1], v[176:179], v[208:211], %[ag1]\n\t"
      "v_rcp_f32 v165, v165\n\t"
      "v_rcp_f32 v166, v166\n\t"
      "v_rcp_f32 v167, v167\n\t"
      "v_rcp_f32 v168, v168\n\t"
      "v_mfma_f32_32x32x16_bf16 %[ag1], v[180:183], v[212:215], %[ag1]\n\t"
      "v_rcp_f32 v169, v169\n\t"
      "ds_read_b64_tr_b16 v[208:209], %[st0] offset:0\n\t"
      "ds_read_b64_tr_b16 v[210:211], %[st1] offset:0\n\t"
      "ds_read_b64_tr_b16 v[212:213], %[st0] offset:1024\n\t"
      "ds_read_b64_tr_b16 v[214:215], %[st1] offset:1024\n\t"
      "v_rcp_f32 v170, v170\n\t"
      "v_rcp_f32 v171, v171\n\t"
      "v_rcp_f32 v172, v172\n\t"
      "v_rcp_f32 v173, v173\n\t"
      "v_rcp_f32 v174, v174\n\t"
      "v_rcp_f32 v175, v175\n\t"
      "v_pk_mul_f32 v[160:161], v[128:129], v[160:161]\n\t"
      "v_pk_mul_f32 v[162:163], v[130:131], v[162:163]\n\t"
      "v_pk_mul_f32 v[164:165], v[132:133], v[164:165]\n\t"
      "v_pk_mul_f32 v[166:167], v[134:135], v[166:167]\n\t"
      "v_pk_mul_f32 v[168:169], v[136:137], v[168:169]\n\t"
      "v_pk_mul_f32 v[170:171], v[138:139], v[170:171]\n\t"
      "v_pk_mul_f32 v[172:173], v[140:141], v[172:173]\n\t"
      "v_pk_mul_f32 v[174:175], v[142:143], v[174:175]\n\t"
      "v_cvt_pk_bf16_f32 v176, v160, v161\n\t"
      "v_cvt_pk_bf16_f32 v177, v162, v163\n\t"
      "v_cvt_pk_bf16_f32 v178, v164, v165\n\t"
      "v_cvt_pk_bf16_f32 v179, v166, v167\n\t"
      "v_cvt_pk_bf16_f32 v180, v168, v169\n\t"
      "v_cvt_pk_bf16_f32 v181, v170, v171\n\t"
      "v_cvt_pk_bf16_f32 v182, v172, v173\n\t"
      "v_cvt_pk_bf16_f32 v183, v174, v175\n\t"
      "s_waitcnt lgkmcnt(4)\n\t"
      "s_cmp_lt_i32 %[k], 1\n\t"
      "s_cbranch_scc1 Lnd6_%=\n\t"
      "s_add_u32 m0, %[ring], 4096\n\t"
      "s_nop 0\n\t"
      "buffer_load_dwordx4 v[216:217], %[srd], 0 idxen offen lds\n\t"
      "s_add_u32 m0, %[ring], 5120\n\t"
      "s_nop 0\n\t"
      "buffer_load_dwordx4 v[218:219], %[srd], 0 idxen offen lds\n\t"
      "s_add_u32 m0, %[ring], 6144\n\t"
      "s_nop 0\n\t"
      "buffer_load_dwordx4 v[220:221], %[srd], 0 idxen offen lds\n\t"
      "s_add_u32 m0, %[ring], 7168\n\t"
      "s_nop 0\n\t"
      "buffer_load_dwordx4 v[222:223], %[srd], 0 idxen offen lds\n\t"
      "s_branch Lge6_%=\n\t"
      "Lnd6_%=:\n\t"
      "s_cmp_lg_i32 %[k], 0\n\t"
      "s_cbranch_scc1 Lge6_%=\n\t"
      "global_load_dwordx4 v[224:227], %[wuo], %[wub] offset:-4096\n\t"
      "global_load_dwordx4 v[228:231], %[wuo], %[wub] offset:-3072\n\t"
      "global_load_dwordx4 v[232:235], %[wuo], %[wub] offset:-2048\n\t"
      "global_load_dwordx4 v[236:239], %[wuo], %[wub] offset:-1024\n\t"
      "global_load_dwordx4 v[240:243], %[wuo], %[wub] offset:0\n\t"
      "global_load_dwordx4 v[244:247], %[wuo], %[wub] offset:1024\n\t"
      "global_load_dwordx4 v[248:251], %[wuo], %[wub] offset:2048\n\t"
      "global_load_dwordx4 v[252:255], %[wuo], %[wub] offset:3072\n\t"
      "Lge6_%=:\n\t"
      "s_sub_i32 %[k], %[k], 1\n\t"
      "v_exp_f32 v160, v144\n\t"
      "v_mfma_f32_32x32x16_bf16 v[128:143], v[200:203], %[q00], 0\n\t"
      "v_exp_f32 v161, v145\n\t"
      "v_exp_f32 v162, v146\n\t"
      "v_exp_f32 v163, v147\n\t"
      "v_exp_f32 v164, v148\n\t"
      "v_mfma_f32_32x32x16_bf16 v[128:143], v[204:207], %[q01], v[128:143]\n\t"
      "v_exp_f32 v165, v149\n\t"
      "v_exp_f32 v166, v150\n\t"
      "v_exp_f32 v167, v151\n\t"
      "v_exp_f32 v168, v152\n\t"
      "v_mfma_f32_32x32x16_bf16 v[128:143], v[184:187], %[id0], v[128:143]\n\t"
      "v_exp_f32 v169, v153\n\t"
      "v_exp_f32 v170, v154\n\t"
      "v_exp_f32 v171, v155\n\t"
      "v_exp_f32 v172, v156\n\t"
      "v_exp_f32 v173, v157\n\t"
      "v_exp_f32 v174, v158\n\t"
      "v_exp_f32 v175, v159\n\t"
      "v_pk_add_f32 v[160:161], v[160:161], 1.0 op_sel_hi:[1,0]\n\t"
      "v_pk_add_f32 v[162:163], v[162:163], 1.0 op_sel_hi:[1,0]\n\t"
      "v_pk_add_f32 v[164:165], v[164:165], 1.0 op_sel_hi:[1,0]\n\t"
      "v_pk_add_f32 v[166:167], v[166:167], 1.0 op_sel_hi:[1,0]\n\t"
      "v_pk_add_f32 v[168:169], v[168:169], 1.0 op_sel_hi:[1,0]\n\t"
      "v_pk_add_f32 v[170:171], v[170:171], 1.0 op_sel_hi:[1,0]\n\t"
      "v_pk_add_f32 v[172:173], v[172:173], 1.0 op_sel_hi:[1,0]\n\t"
      "v_pk_add_f32 v[174:175], v[174:175], 1.0 op_sel_hi:[1,0]\n\t"
      "v_rcp_f32 v160, v160\n\t"
      "v_mfma_f32_32x32x16_bf16 v[128:143], v[188:191], %[id1], v[128:143]\n\t"
      "v_rcp_f32 v161, v161\n\t"
      "s_waitcnt lgkmcnt(0)\n\t"
      "v_rcp_f32 v162, v162\n\t"
      "v_rcp_f32 v163, v163\n\t"
      "v_rcp_f32 v164, v164\n\t"
      "v_mfma_f32_32x32x16_bf16 %[ag0], v[176:179], v[208:211], %[ag0]\n\t"
      "v_rcp_f32 v165, v165\n\t"
      "v_rcp_f32 v166, v166\n\t"
      "v_rcp_f32 v167, v167\n\t"
      "v_rcp_f32 v168, v168\n\t"
      "v_mfma_f32_32x32x16_bf16 %[ag0], v[180:183], v[212:215], %[ag0]\n\t"
      "v_rcp_f32 v169, v169\n\t"
      "v_rcp_f32 v170, v170\n\t"
      "v_rcp_f32 v171, v171\n\t"
      "v_rcp_f32 v172, v172\n\t"
      "v_rcp_f32 v173, v173\n\t"
      "v_rcp_f32 v174, v174\n\t"
      "v_rcp_f32 v175, v175\n\t"
      "v_pk_mul_f32 v[160:161], v[144:145], v[160:161]\n\t"
      "v_pk_mul_f32 v[162:163], v[146:147], v[162:163]\n\t"
      "v_pk_mul_f32 v[164:165], v[148:149], v[164:165]\n\t"
      "v_pk_mul_f32 v[166:167], v[150:151], v[166:167]\n\t"
      "v_pk_mul_f32 v[168:169], v[152:153], v[168:169]\n\t"
      "v_pk_mul_f32 v[170:171], v[154:155], v[170:171]\n\t"
      "v_pk_mul_f32 v[172:173], v[156:157], v[172:173]\n\t"
      "v_pk_mul_f32 v[174:175], v[158:159], v[174:175]\n\t"
      "v_cvt_pk_bf16_f32 v176, v160, v161\n\t"
      "v_cvt_pk_bf16_f32 v177, v162, v163\n\t"
      "v_cvt_pk_bf16_f32 v178, v164, v165\n\t"
      "v_cvt_pk_bf16_f32 v179, v166, v167\n\t"
      "v_cvt_pk_bf16_f32 v180, v168, v169\n\t"
      "v_cvt_pk_bf16_f32 v181, v170, v171\n\t"
      "v_cvt_pk_bf16_f32 v182, v172, v173\n\t"
      "v_cvt_pk_bf16_f32 v183, v174, v175\n\t"
      "s_sub_u32 %[t], %[t], 1\n\t"
      "s_cmp_eq_u32 %[t], 0\n\t"
      "s_cbranch_scc1 Lexit_even_%=\n\t"
      "v_exp_f32 v160, v128\n\t"
      "v_mfma_f32_32x32x16_bf16 v[144:159], v[200:203], %[q10], 0\n\t"
      "v_exp_f32 v161, v129\n\t"
      "v_exp_f32 v162, v130\n\t"
      "v_exp_f32 v163, v131\n\t"
      "v_exp_f32 v164, v132\n\t"
      "v_mfma_f32_32x32x16_bf16 v[144:159], v[204:207], %[q11], v[144:159]\n\t"
      "v_exp_f32 v165, v133\n\t"
      "s_cmp_lt_i32 %[k], 0\n\t"
      "s_cbranch_scc1 Lw87_%=\n\t"
      "s_waitcnt vmcnt(4)\n\t"
      "Lw87_%=:\n\t"
      "s_waitcnt vmcnt(8)\n\t"
      "ds_read_b128 v[200:203], %[sa0] offset:4096\n\t"
      "ds_read_b128 v[204:207], %[sa1] offset:4096\n\t"
      "ds_read_b128 v[184:187], %[pa0] offset:0\n\t"
      "ds_read_b128 v[188:191], %[pa1] offset:0\n\t"
      "v_exp_f32 v166, v134\n\t"
      "v_exp_f32 v167, v135\n\t"
      "v_exp_f32 v168, v136\n\t"
      "v_mfma_f32_32x32x16_bf16 v[144:159], v[192:195], %[id0], v[144:159]\n\t"
      "v_exp_f32 v169, v137\n\t"
      "v_exp_f32 v170, v138\n\t"
      "v_exp_f32 v171, v139\n\t"
      "v_exp_f32 v172, v140\n\t"
      "v_exp_f32 v173, v141\n\t"
      "v_exp_f32 v174, v142\n\t"
      "v_exp_f32 v175, v143\n\t"
      "v_pk_add_f32 v[160:161], v[160:161], 1.0 op_sel_hi:[1,0]\n\t"
      "v_pk_add_f32 v[162:163], v[162:163], 1.0 op_sel_hi:[1,0]\n\t"
      "v_pk_add_f32 v[164:165], v[164:165], 1.0 op_sel_hi:[1,0]\n\t"
      "v_pk_add_f32 v[166:167], v[166:167], 1.0 op_sel_hi:[1,0]\n\t"
      "v_pk_add_f32 v[168:169], v[168:169], 1.0 op_sel_hi:[1,0]\n\t"
      "v_pk_add_f32 v[170:171], v[170:171], 1.0 op_sel_hi:[1,0]\n\t"
      "v_pk_add_f32 v[172:173], v[172:173], 1.0 op_sel_hi:[1,0]\n\t"
      "v_pk_add_f32 v[174:175], v[174:175], 1.0 op_sel_hi:[1,0]\n\t"
      "v_rcp_f32 v160, v160\n\t"
      "v_mfma_f32_32x32x16_bf16 v[144:159], v[196:199], %[id1], v[144:159]\n\t"
      "v_rcp_f32 v161, v161\n\t"
      "ds_read_b128 v[192:195], %[pa2] offset:0\n\t"
      "ds_read_b128 v[196:199], %[pa3] offset:0\n\t"
      "ds_read_b32 v216, %[lst] offset:512\n\t"
      "ds_read_b32 v218, %[lst] offset:544\n\t"
      "ds_read_b32 v220, %[lst] offset:576\n\t"
      "ds_read_b32 v222, %[lst] offset:608\n\t"
      "v_rcp_f32 v162, v162\n\t"
      "v_rcp_f32 v163, v163\n\t"
      "v_rcp_f32 v164, v164\n\t"
      "v_mfma_f32_32x32x16_bf16 %[ag1], v[176:179], v[208:211], %[ag1]\n\t"
      "v_rcp_f32 v165, v165\n\t"
      "v_rcp_f32 v166, v166\n\t"
      "v_rcp_f32 v167, v167\n\t"
      "v_rcp_f32 v168, v168\n\t"
      "v_mfma_f32_32x32x16_bf16 %[ag1], v[180:183], v[212:215], %[ag1]\n\t"
      "v_rcp_f32 v169, v169\n\t"
      "ds_read_b64_tr_b16 v[208:209], %[st0] offset:2048\n\t"
      "ds_read_b64_tr_b16 v[210:211], %[st1] offset:2048\n\t"
      "ds_read_b64_tr_b16 v[212:213], %[st0] offset:3072\n\t"
      "ds_read_b64_tr_b16 v[214:215], %[st1] offset:3072\n\t"
      "v_rcp_f32 v170, v170\n\t"
      "v_rcp_f32 v171, v171\n\t"
      "v_rcp_f32 v172, v172\n\t"
      "v_rcp_f32 v173, v173\n\t"
      "v_rcp_f32 v174, v174\n\t"
      "v_rcp_f32 v175, v175\n\t"
      "v_pk_mul_f32 v[160:161], v[128:129], v[160:161]\n\t"
      "v_pk_mul_f32 v[162:163], v[130:131], v[162:163]\n\t"
      "v_pk_mul_f32 v[164:165], v[132:133], v[164:165]\n\t"
      "v_pk_mul_f32 v[166:167], v[134:135], v[166:167]\n\t"
      "v_pk_mul_f32 v[168:169], v[136:137], v[168:169]\n\t"
      "v_pk_mul_f32 v[170:171], v[138:139], v[170:171]\n\t"
      "v_pk_mul_f32 v[172:173], v[140:141], v[172:173]\n\t"
      "v_pk_mul_f32 v[174:175], v[142:143], v[174:175]\n\t"
      "v_cvt_pk_bf16_f32 v176, v160, v161\n\t"
      "v_cvt_pk_bf16_f32 v177, v162, v163\n\t"
      "v_cvt_pk_bf16_f32 v178, v164, v165\n\t"
      "v_cvt_pk_bf16_f32 v179, v166, v167\n\t"
      "v_cvt_pk_bf16_f32 v180, v168, v169\n\t"
      "v_cvt_pk_bf16_f32 v181, v170, v171\n\t"
      "v_cvt_pk_bf16_f32 v182, v172, v173\n\t"
      "v_cvt_pk_bf16_f32 v183, v174, v175\n\t"
      "s_waitcnt lgkmcnt(4)\n\t"
      "s_cmp_lt_i32 %[k], 1\n\t"
      "s_cbranch_scc1 Lnd8_%=\n\t"
      "s_add_u32 m0, %[ring], 0\n\t"
      "s_nop 0\n\t"
      "buffer_load_dwordx4 v[216:217], %[srd], 0 idxen offen lds\n\t"
      "s_add_u32 m0, %[ring], 1024\n\t"
      "s_nop 0\n\t"
      "buffer_load_dwordx4 v[218:219], %[srd], 0 idxen offen lds\n\t"
      "s_add_u32 m0, %[ring], 2048\n\t"
      "s_nop 0\n\t"
      "buffer_load_dwordx4 v[220:221], %[srd], 0 idxen offen lds\n\t"
      "s_add_u32 m0, %[ring], 3072\n\t"
      "s_nop 0\n\t"
      "buffer_load_dwordx4 v[222:223], %[srd], 0 idxen offen lds\n\t"
      "s_branch Lge8_%=\n\t"
      "Lnd8_%=:\n\t"
      "s_cmp_lg_i32 %[k], 0\n\t"
      "s_cbranch_scc1 Lge8_%=\n\t"
      "global_load_dwordx4 v[224:227], %[wuo], %[wub] offset:-4096\n\t"
      "global_load_dwordx4 v[228:231], %[wuo], %[wub] offset:-3072\n\t"
      "global_load_dwordx4 v[232:235], %[wuo], %[wub] offset:-2048\n\t"
      "global_load_dwordx4 v[236:239], %[wuo], %[wub] offset:-1024\n\t"
      "global_load_dwordx4 v[240:243], %[wuo], %[wub] offset:0\n\t"
      "global_load_dwordx4 v[244:247], %[wuo], %[wub] offset:1024\n\t"
      "global_load_dwordx4 v[248:251], %[wuo], %[wub] offset:2048\n\t"
      "global_load_dwordx4 v[252:255], %[wuo], %[wub] offset:3072\n\t"
      "Lge8_%=:\n\t"
      "s_sub_i32 %[k], %[k], 1\n\t"
      "v_exp_f32 v160, v144\n\t"
      "v_mfma_f32_32x32x16_bf16 v[128:143], v[200:203], %[q00], 0\n\t"
      "v_exp_f32 v161, v145\n\t"
      "v_exp_f32 v162, v146\n\t"
      "v_exp_f32 v163, v147\n\t"
      "v_exp_f32 v164, v148\n\t"
      "v_mfma_f32_32x32x16_bf16 v[128:143], v[204:207], %[q01], v[128:143]\n\t"
      "v_exp_f32 v165, v149\n\t"
      "v_exp_f32 v166, v150\n\t"
      "v_exp_f32 v167, v151\n\t"
      "v_exp_f32 v168, v152\n\t"
      "v_mfma_f32_32x32x16_bf16 v[128:143], v[184:187], %[id0], v[128:143]\n\t"
      "v_exp_f32 v169, v153\n\t"
      "v_exp_f32 v170, v154\n\t"
      "v_exp_f32 v171, v155\n\t"
      "v_exp_f32 v172, v156\n\t"
      "v_exp_f32 v173, v157\n\t"
      "v_exp_f32 v174, v158\n\t"
      "v_exp_f32 v175, v159\n\t"
      "v_pk_add_f32 v[160:161], v[160:161], 1.0 op_sel_hi:[1,0]\n\t"
      "v_pk_add_f32 v[162:163], v[162:163], 1.0 op_sel_hi:[1,0]\n\t"
      "v_pk_add_f32 v[164:165], v[164:165], 1.0 op_sel_hi:[1,0]\n\t"
      "v_pk_add_f32 v[166:167], v[166:167], 1.0 op_sel_hi:[1,0]\n\t"
      "v_pk_add_f32 v[168:169], v[168:169], 1.0 op_sel_hi:[1,0]\n\t"
      "v_pk_add_f32 v[170:171], v[170:171], 1.0 op_sel_hi:[1,0]\n\t"
      "v_pk_add_f32 v[172:173], v[172:173], 1.0 op_sel_hi:[1,0]\n\t"
      "v_pk_add_f32 v[174:175], v[174:175], 1.0 op_sel_hi:[1,0]\n\t"
      "v_rcp_f32 v160, v160\n\t"
      "v_mfma_f32_32x32x16_bf16 v[128:143], v[188:191], %[id1], v[128:143]\n\t"
      "v_rcp_f32 v161, v161\n\t"
      "s_waitcnt lgkmcnt(0)\n\t"
      "v_rcp_f32 v162, v162\n\t"
      "v_rcp_f32 v163, v163\n\t"
      "v_rcp_f32 v164, v164\n\t"
      "v_mfma_f32_32x32x16_bf16 %[ag0], v[176:179], v[208:211], %[ag0]\n\t"
      "v_rcp_f32 v165, v165\n\t"
      "v_rcp_f32 v166, v166\n\t"
      "v_rcp_f32 v167, v167\n\t"
      "v_rcp_f32 v168, v168\n\t"
      "v_mfma_f32_32x32x16_bf16 %[ag0], v[180:183], v[212:215], %[ag0]\n\t"
      "v_rcp_f32 v169, v169\n\t"
      "v_rcp_f32 v170, v170\n\t"
      "v_rcp_f32 v171, v171\n\t"
      "v_rcp_f32 v172, v172\n\t"
      "v_rcp_f32 v173, v173\n\t"
      "v_rcp_f32 v174, v174\n\t"
      "v_rcp_f32 v175, v175\n\t"
      "v_pk_mul_f32 v[160:161], v[144:145], v[160:161]\n\t"
      "v_pk_mul_f32 v[162:163], v[146:147], v[162:163]\n\t"
      "v_pk_mul_f32 v[164:165], v[148:149], v[164:165]\n\t"
      "v_pk_mul_f32 v[166:167], v[150:151], v[166:167]\n\t"
      "v_pk_mul_f32 v[168:169], v[152:153], v[168:169]\n\t"
      "v_pk_mul_f32 v[170:171], v[154:155], v[170:171]\n\t"
      "v_pk_mul_f32 v[172:173], v[156:157], v[172:173]\n\t"
      "v_pk_mul_f32 v[174:175], v[158:159], v[174:175]\n\t"
      "v_cvt_pk_bf16_f32 v176, v160, v161\n\t"
      "v_cvt_pk_bf16_f32 v177, v162, v163\n\t"
      "v_cvt_pk_bf16_f32 v178, v164, v165\n\t"
      "v_cvt_pk_bf16_f32 v179, v166, v167\n\t"
      "v_cvt_pk_bf16_f32 v180, v168, v169\n\t"
      "v_cvt_pk_bf16_f32 v181, v170, v171\n\t"
      "v_cvt_pk_bf16_f32 v182, v172, v173\n\t"
      "v_cvt_pk_bf16_f32 v183, v174, v175\n\t"
      "v_add_u32 %[sa0], 0x1000, %[sa0]\n\t"
      "v_add_u32 %[sa1], 0x1000, %[sa1]\n\t"
      "v_add_u32 %[st0], 0x1000, %[st0]\n\t"
      "v_add_u32 %[st1], 0x1000, %[st1]\n\t"
      "v_add_u32 %[lst], 0x100, %[lst]\n\t"
      "s_sub_u32 %[t], %[t], 1\n\t"
      "s_cmp_lg_u32 %[t], 0\n\t"
      "s_cbranch_scc1 Lloop_%=\n\t"
      "s_branch Ltail_%=\n\t"
      "Lexit_even_%=:\n\t"
      "v_add_u32 %[st0], 0x800, %[st0]\n\t"
      "v_add_u32 %[st1], 0x800, %[st1]\n\t"
      "Ltail_%=:\n\t"
      "s_nop 1\n\t"
      "v_mfma_f32_32x32x16_bf16 %[ag1], v[176:179], v[208:211], %[ag1]\n\t"
      "v_mfma_f32_32x32x16_bf16 %[ag1], v[180:183], v[212:215], %[ag1]\n\t"
      "s_cmp_eq_u32 %[m], 0\n\t"
      "s_cbranch_scc1 Ldone_%=\n\t"
      "ds_read_b64_tr_b16 v[208:209], %[st0] offset:0\n\t"
      "ds_read_b64_tr_b16 v[210:211], %[st1] offset:0\n\t"
      "ds_read_b64_tr_b16 v[212:213], %[st0] offset:1024\n\t"
      "ds_read_b64_tr_b16 v[214:215], %[st1] offset:1024\n\t"
      "v_mfma_f32_32x32x16_bf16 v[144:159], v[200:203], %[q10], 0\n\t"
      "v_mfma_f32_32x32x16_bf16 v[144:159], v[204:207], %[q11], v[144:159]\n\t"
      "v_mov_b32 v178, 0\n\t"
      "v_mov_b32 v179, 0\n\t"
      "v_mov_b32 v180, 0\n\t"
      "v_mov_b32 v181, 0\n\t"
      "v_mov_b32 v182, 0\n\t"
      "v_mov_b32 v183, 0\n\t"
      "v_mfma_f32_32x32x16_bf16 v[144:159], v[192:195], %[id0], v[144:159]\n\t"
      "v_mfma_f32_32x32x16_bf16 v[144:159], v[196:199], %[id1], v[144:159]\n\t"
      "v_exp_f32 v160, v128\n\t"
      "v_exp_f32 v161, v129\n\t"
      "v_exp_f32 v162, v130\n\t"
      "v_exp_f32 v163, v131\n\t"
      "s_nop 0\n\t"
      "v_add_f32 v160, 1.0, v160\n\t"
      "v_add_f32 v161, 1.0, v161\n\t"
      "v_add_f32 v162, 1.0, v162\n\t"
      "v_add_f32 v163, 1.0, v163\n\t"
      "v_rcp_f32 v160, v160\n\t"
      "v_rcp_f32 v161, v161\n\t"
      "v_rcp_f32 v162, v162\n\t"
      "v_rcp_f32 v163, v163\n\t"
      "s_nop 0\n\t"
      "v_mul_f32 v160, v128, v160\n\t"
      "v_mul_f32 v161, v129, v161\n\t"
      "v_mul_f32 v162, v130, v162\n\t"
      "v_mul_f32 v163, v131, v163\n\t"
      "v_cvt_pk_bf16_f32 v176, v160, v161\n\t"
      "v_cvt_pk_bf16_f32 v177, v162, v163\n\t"
      "s_cmp_lt_u32 %[m], 2\n\t"
      "s_cbranch_scc1 Lqa_%=\n\t"
      "v_exp_f32 v164, v132\n\t"
      "v_exp_f32 v165, v133\n\t"
      "v_exp_f32 v166, v134\n\t"
      "v_exp_f32 v167, v135\n\t"
      "s_nop 0\n\t"
      "v_add_f32 v164, 1.0, v164\n\t"
      "v_add_f32 v165, 1.0, v165\n\t"
      "v_add_f32 v166, 1.0, v166\n\t"
      "v_add_f32 v167, 1.0, v167\n\t"
      "v_rcp_f32 v164, v164\n\t"
      "v_rcp_f32 v165, v165\n\t"
      "v_rcp_f32 v166, v166\n\t"
      "v_rcp_f32 v167, v167\n\t"
      "s_nop 0\n\t"
      "v_mul_f32 v164, v132, v164\n\t"
      "v_mul_f32 v165, v133, v165\n\t"
      "v_mul_f32 v166, v134, v166\n\t"
      "v_mul_f32 v167, v135, v167\n\t"
      "v_cvt_pk_bf16_f32 v178, v164, v165\n\t"
      "v_cvt_pk_bf16_f32 v179, v166, v167\n\t"
      "s_cmp_lt_u32 %[m], 3\n\t"
      "s_cbranch_scc1 Lqa_%=\n\t"
      "v_exp_f32 v168, v136\n\t"
      "v_exp_f32 v169, v137\n\t"
      "v_exp_f32 v170, v138\n\t"
      "v_exp_f32 v171, v139\n\t"
      "s_nop 0\n\t"
      "v_add_f32 v168, 1.0, v168\n\t"
      "v_add_f32 v169, 1.0, v169\n\t"
      "v_add_f32 v170, 1.0, v170\n\t"
      "v_add_f32 v171, 1.0, v171\n\t"
      "v_rcp_f32 v168, v168\n\t"
      "v_rcp_f32 v169, v169\n\t"
      "v_rcp_f32 v170, v170\n\t"
      "v_rcp_f32 v171, v171\n\t"
      "s_nop 0\n\t"
      "v_mul_f32 v168, v136, v168\n\t"
      "v_mul_f32 v169, v137, v169\n\t"
      "v_mul_f32 v170, v138, v170\n\t"
      "v_mul_f32 v171, v139, v171\n\t"
      "v_cvt_pk_bf16_f32 v180, v168, v169\n\t"
      "v_cvt_pk_bf16_f32 v181, v170, v171\n\t"
      "Lqa_%=:\n\t"
      "s_waitcnt lgkmcnt(0)\n\t"
      "s_nop 1\n\t"
      "v_mfma_f32_32x32x16_bf16 %[ag0], v[176:179], v[208:211], %[ag0]\n\t"
      "v_mfma_f32_32x32x16_bf16 %[ag0], v[180:183], v[212:215], %[ag0]\n\t"
      "v_exp_f32 v160, v144\n\t"
      "v_exp_f32 v161, v145\n\t"
      "v_exp_f32 v162, v146\n\t"
      "v_exp_f32 v163, v147\n\t"
      "s_nop 0\n\t"
      "v_add_f32 v160, 1.0, v160\n\t"
      "v_add_f32 v161, 1.0, v161\n\t"
      "v_add_f32 v162, 1.0, v162\n\t"
      "v_add_f32 v163, 1.0, v163\n\t"
      "v_rcp_f32 v160, v160\n\t"
      "v_rcp_f32 v161, v161\n\t"
      "v_rcp_f32 v162, v162\n\t"
      "v_rcp_f32 v163, v163\n\t"
      "s_nop 0\n\t"
      "v_mul_f32 v160, v144, v160\n\t"
      "v_mul_f32 v161, v145, v161\n\t"
      "v_mul_f32 v162, v146, v162\n\t"
      "v_mul_f32 v163, v147, v163\n\t"
      "v_cvt_pk_bf16_f32 v176, v160, v161\n\t"
      "v_cvt_pk_bf16_f32 v177, v162, v163\n\t"
      "s_cmp_lt_u32 %[m], 2\n\t"
      "s_cbranch_scc1 Lqb_%=\n\t"
      "v_exp_f32 v164, v148\n\t"
      "v_exp_f32 v165, v149\n\t"
      "v_exp_f32 v166, v150\n\t"
      "v_exp_f32 v167, v151\n\t"
      "s_nop 0\n\t"
      "v_add_f32 v164, 1.0, v164\n\t"
      "v_add_f32 v165, 1.0, v165\n\t"
      "v_add_f32 v166, 1.0, v166\n\t"
      "v_add_f32 v167, 1.0, v167\n\t"
      "v_rcp_f32 v164, v164\n\t"
      "v_rcp_f32 v165, v165\n\t"
      "v_rcp_f32 v166, v166\n\t"
      "v_rcp_f32 v167, v167\n\t"
      "s_nop 0\n\t"
      "v_mul_f32 v164, v148, v164\n\t"
      "v_mul_f32 v165, v149, v165\n\t"
      "v_mul_f32 v166, v150, v166\n\t"
      "v_mul_f32 v167, v151, v167\n\t"
      "v_cvt_pk_bf16_f32 v178, v164, v165\n\t"
      "v_cvt_pk_bf16_f32 v179, v166, v167\n\t"
      "s_cmp_lt_u32 %[m], 3\n\t"
      "s_cbranch_scc1 Lqb_%=\n\t"
      "v_exp_f32 v168, v152\n\t"
      "v_exp_f32 v169, v153\n\t"
      "v_exp_f32 v170, v154\n\t"
      "v_exp_f32 v171, v155\n\t"
      "s_nop 0\n\t"
      "v_add_f32 v168, 1.0, v168\n\t"
      "v_add_f32 v169, 1.0, v169\n\t"
      "v_add_f32 v170, 1.0, v170\n\t"
      "v_add_f32 v171, 1.0, v171\n\t"
      "v_rcp_f32 v168, v168\n\t"
      "v_rcp_f32 v169, v169\n\t"
      "v_rcp_f32 v170, v170\n\t"
      "v_rcp_f32 v171, v171\n\t"
      "s_nop 0\n\t"
      "v_mul_f32 v168, v152, v168\n\t"
      "v_mul_f32 v169, v153, v169\n\t"
      "v_mul_f32 v170, v154, v170\n\t"
      "v_mul_f32 v171, v155, v171\n\t"
      "v_cvt_pk_bf16_f32 v180, v168, v169\n\t"
      "v_cvt_pk_bf16_f32 v181, v170, v171\n\t"
      "Lqb_%=:\n\t"
      "s_nop 1\n\t"
      "v_mfma_f32_32x32x16_bf16 %[ag1], v[176:179], v[208:211], %[ag1]\n\t"
      "v_mfma_f32_32x32x16_bf16 %[ag1], v[180:183], v[212:215], %[ag1]\n\t"
      "Ldone_%=:\n\t"
      "s_mov_b32 m0, %[keep]\n\t"
      "s_waitcnt vmcnt(0) lgkmcnt(0)\n\t"
      "s_nop 11\n\t"
      : [ag0] "+v"(agg0), [ag1] "+v"(agg1), "=&{v[224:239]}"(wu0), "=&{v[240:255]}"(wu1), [sa0] "+v"(sa0), [sa1] "+v"(sa1),
        [st0] "+v"(st0), [st1] "+v"(st1), [lst] "+v"(lst), [k] "+s"(k), [t] "=&s"(t), [keep] "=&s"(keep)
      : [q00] "v"(q00), [q01] "v"(q01), [q10] "v"(q10), [q11] "v"(q11), [id0] "v"(id0), [id1] "v"(id1), [pa0] "v"(pa0),
        [pa1] "v"(pa0 ^ 32u), [pa2] "v"(pa0 ^ 64u), [pa3] "v"(pa0 ^ 96u), [s0] "v"(s0), [s1] "v"(s0 ^ 64u), [wuo] "v"(wuo),
        [srd] "s"(srd), [wub] "s"(wub), [ring] "s"(ring), [nf] "s"(nf), [m] "s"(m)
      : "memory", "scc", "v128", "v129", "v130", "v131", "v132", "v133", "v134", "v135", "v136", "v137", "v138", "v139", "v140", "v141", "v142", "v143", "v144", "v145", "v146", "v147", "v148", "v149", "v150", "v151", "v152", "v153", "v154", "v155", "v156", "v157", "v158", "v159", "v160", "v161", "v162", "v163", "v164", "v165", "v166", "v167", "v168", "v169", "v170", "v171", "v172", "v173", "v174", "v175", "v176", "v177", "v178", "v179", "v180", "v181", "v182", "v183", "v184", "v185", "v186", "v187", "v188", "v189", "v190", "v191", "v192", "v193", "v194", "v195", "v196", "v197", "v198", "v199", "v200", "v201", "v202", "v203", "v204", "v205", "v206", "v207", "v208", "v209", "v210", "v211", "v212", "v213", "v214", "v215", "v216", "v217", "v218", "v219", "v220", "v221", "v222", "v223");
}

DI void put_record(char* lds, int sub, unsigned row, Rec r) {
  const unsigned rr = sub * CAP + row, sw = (rr >> 2) & 3u, k = r.a & 255u;
  char* srow = lds + L_S + rr * 64;
  *(unsigned*)(lds + L_LIST + rr * 4) = r.a >> 8;
  *(unsigned short*)(srow + (((k >> 3) ^ sw) << 4) + ((k & 7u) << 1)) = 0x3F80;
  u32x4 e = {r.e01, r.e23, r.e01, r.e23};
  *(u32x4*)(srow + ((3u ^ sw) << 4)) = e;
}

DI void clear_images(char* lds, int tid) {
  const u32x4 z = {0, 0, 0, 0}, o = {OOB, OOB, OOB, OOB};
#pragma unroll
  for (int i = 0; i < (L_LIST / 16 + 511) / 512; ++i)
    if (tid + i * 512 < L_LIST / 16) *(u32x4*)(lds + (tid + i * 512) * 16) = z;
  if (tid < (L_ZEND - L_LIST) / 16) *(u32x4*)(lds + L_LIST + tid * 16) = o;
}

__global__ __launch_bounds__(512, 2) void mpnn_edge(const unsigned short* __restrict__ x, const char* __restrict__ ws,
                                                    unsigned short* __restrict__ out) {
  __shared__ __attribute__((aligned(16))) char lds[L_TOTAL];
  const int tid = threadIdx.x, lane = tid & 63, w = __builtin_amdgcn_readfirstlane(tid >> 6);
  const int g = blockIdx.x, ord = (g & 7) * 32 + (g >> 3);
  const int sub = w >> 2, fh = w & 1, half = (w >> 1) & 1, w4 = w & 3;
  const int c = lane & 31, h = lane >> 5, pc = w4 * 16 + (lane >> 2);
  STAMP_DECL
  STAMP(0);

  const u32x2 tb = *(const u32x2*)(ws + WS_TAB + pc * 2048 + ord * 8);
  const unsigned ring = lds_addr(lds) + L_RING + w * 8192;
  {
    const u32x4 xs = make_srd(x, NN * 256), ws2 = make_srd(ws + WS_W2, 32768);
    const unsigned r4 = lane >> 4, c16 = lane & 15;
    unsigned row = w * 4 + r4;
    dma16(xs, (row * 256 + g) * 256 + ((c16 ^ (row & 15)) << 4), lds_addr(lds) + L_X + w * 1024);
    if (w < 4) {
      row += 32;
      dma16(xs, (row * 256 + g) * 256 + ((c16 ^ (row & 15)) << 4), lds_addr(lds) + L_X + (w + 8) * 1024);
    }
#pragma unroll
    for (int i = 0; i < 4; ++i) dma16(ws2, (w * 4 + i) * 1024 + lane * 16, ring + 4096 + i * 1024);
  }
  const int fbq = 2 * fh + half;
  if (w == 7) dma16(make_srd(out, NN * 256), (lane * 256 + g) * 256, ring);
  const u32x4 w3 = *(const u32x4*)(ws + WS_W3 + (fbq * 32 + c) * 16);
  clear_images(lds, tid);
  STAMP(1);

  const unsigned sA = tb[0] & 0xffffu, sB = tb[0] >> 16, eB = tb[1];
  const unsigned first = sub ? sB : sA, cnt = sub ? eB - sB : sB - sA, l4 = lane & 3;
  const Rec* rp = (const Rec*)(ws + WS_REC) + pc * CHUNK + first;
  Rec rec[NREC];
#pragma unroll
  for (int i = 0; i < NREC; ++i) {
    rec[i] = Rec{0, 0, 0};
    if (l4 + 4 * i < cnt) rec[i] = rp[l4 + 4 * i];
  }
  const unsigned incl = wave_scan(l4 == 0 ? cnt : 0u);
  unsigned* wsum = (unsigned*)(lds + L_WSUM);
  if (lane == 63) wsum[w] = incl;
  STAMP(2);
  barrier_all();
  STAMP(3);
  const u32x4 wa = *(const u32x4*)wsum, wb = *(const u32x4*)(wsum + 4);
  const unsigned nA = wa[0] + wa[1] + wa[2] + wa[3], nB = wb[0] + wb[1] + wb[2] + wb[3];
  const u32x4 wm = sub ? wb : wa;
  const unsigned pos = (w4 > 0 ? wm[0] : 0) + (w4 > 1 ? wm[1] : 0) + (w4 > 2 ? wm[2] : 0) + incl - cnt;
  const int ntot = __builtin_amdgcn_readfirstlane(sub ? nB : nA);
  const int nmax = __builtin_amdgcn_readfirstlane(max(nA, nB));
#pragma unroll
  for (int i = 0; i < NREC; ++i)
    if (l4 + 4 * i < cnt && pos + l4 + 4 * i < CAP) put_record(lds, sub, pos + l4 + 4 * i, rec[i]);
  for (unsigned i = l4 + 4 * NREC; i < cnt && pos + i < CAP; i += 4) put_record(lds, sub, pos + i, rp[i]);

  bf16x8 qf[2][2];
  {
    const int xrow = sub * SUBN + min(c, SUBN - 1);
    const char* wq = lds + L_RING + 4096 + fbq * 16384 + lane * 16;
    f32x16 q = {};
#pragma unroll
    for (int s = 0; s < 8; ++s)
      q = mfma32(lds_frag(lds + L_X + 256 * xrow + 16 * ((2 * s + h) ^ (xrow & 15))),
                 lds_frag(wq + (s >> 2) * 8192 + (s & 3) * 1024), q);
    u32x4 a, b;
#pragma unroll
    for (int i = 0; i < 4; ++i) {
      a[i] = pack2(q[2 * i] * NEG_LOG2E, q[2 * i + 1] * NEG_LOG2E);
      b[i] = pack2(q[8 + 2 * i] * NEG_LOG2E, q[9 + 2 * i] * NEG_LOG2E);
      b[i] = h ? w3[i] : b[i];
    }
    char* xq = lds + L_RING + (w ^ 2) * 8192 + lane * 16;
    *(u32x4*)xq = a;
    *(u32x4*)(xq + 1024) = b;
    STAMP(4);
    barrier_lds();
    STAMP(5);
    const char* rq = lds + L_RING + w * 8192 + lane * 16;
    const u32x4 oa = *(const u32x4*)rq, ob = *(const u32x4*)(rq + 1024);
    qf[0][0] = as_frag(half ? oa : a);
    qf[0][1] = as_frag(half ? ob : b);
    qf[1][0] = as_frag(half ? a : oa);
    qf[1][1] = as_frag(half ? b : ob);
    asm volatile("s_waitcnt lgkmcnt(0)" : "+v"(qf[0][0]), "+v"(qf[0][1]), "+v"(qf[1][0]), "+v"(qf[1][1])::"memory");
  }

  bf16x8 id[2];
#pragma unroll
  for (int s = 0; s < 2; ++s) {
    u32x4 v;
#pragma unroll
    for (int i = 0; i < 4; ++i) {
      const int f0 = ((2 * i) & 3) + 16 * h + 8 * (i >> 1) + 4 * s;
      v[i] = (c == f0 ? 0x3F80u : 0u) | (c == f0 + 1 ? 0x3F800000u : 0u);
    }
    id[s] = as_frag(v);
  }

  u32x4 srd = make_srd(ws + WS_P, P_BYTES / 256);
  srd[1] |= 256u << 16;
  const unsigned s0 = fh * 128 + (((lane & 7) ^ (lane >> 4)) << 4);
  const unsigned pa0 = ring + c * 128 + ((h ^ ((c >> 1) & 7)) << 4);
  const unsigned lb = lds_addr(lds);
  u32x16 wu0, wu1;

  f32x16 agg0 = {}, agg1 = {};
  for (int base = 0;;) {
    const int nw = min(max(ntot - base, 0), CAP), oct = (nw + 7) >> 3;
    const int o0 = sub ? oct >> 1 : (oct + 1) >> 1;
    const int myo = half ? oct - o0 : o0, r0 = sub * CAP + (half ? o0 * 8 : 0);
    const int sr = r0 + c;
    const unsigned sa0 = lb + L_S + sr * 64 + ((h ^ ((sr >> 2) & 3)) << 4);
    const int tq = (lane & 15) >> 2, tp = ((lane & 1) << 1) | ((lane >> 1) & 1), tch = 2 * ((lane >> 4) & 1) + (tp >> 1);
    const int tr0 = r0 + 4 * h + tq, tr1 = tr0 + 8;
    const unsigned st0 = lb + L_S + tr0 * 64 + 16 * (tch ^ ((tr0 >> 2) & 3)) + 8 * (tp & 1);
    const unsigned st1 = lb + L_S + tr1 * 64 + 16 * (tch ^ ((tr1 >> 2) & 3)) + 8 * (tp & 1);
    STAMP(6);
    edge_tiles(agg0, agg1, wu0, wu1, qf[0][0], qf[0][1], qf[1][0], qf[1][1], id[0], id[1], pa0, sa0, sa0 ^ 32u, st0, st1,
               lb + L_LIST + (r0 + (lane >> 3)) * 4, s0, w * 8192 + lane * 16, srd, ws + WS_WU + 4096, ring, myo >> 2, myo & 3);
    base += CAP;
    if (base >= nmax) break;
    barrier_all();
    clear_images(lds, tid);
    barrier_lds();
    const unsigned skip = pos < (unsigned)base ? (base - pos) & ~3u : 0u;
    for (unsigned i = skip + l4; i < cnt && pos + i < (unsigned)(base + CAP); i += 4)
      if (pos + i >= (unsigned)base) put_record(lds, sub, pos + i - base, rp[i]);
    barrier_all();
  }
  STAMP(8);

  const f32x16 give = half ? agg0 : agg1, mine = half ? agg1 : agg0;
#pragma unroll
  for (int q = 0; q < 4; ++q)
    *(f32x4*)(lds + L_RING + w * 8192 + q * 1024 + lane * 16) =
        (f32x4){give[4 * q], give[4 * q + 1], give[4 * q + 2], give[4 * q + 3]};
  const int n16 = lane & 15, kq = lane >> 4;
  f32x4 z[3] = {};
#pragma unroll
  for (int ct = 0; ct < 3; ++ct) {
#pragma unroll
    for (int s = 0; s < 4; ++s)
      z[ct] = mfma16(sub_frag(wu0, s), lds_frag(lds + L_X + 256 * (16 * ct + n16) + 16 * ((4 * s + kq) ^ n16)), z[ct]);
    z[ct] *= NEG_LOG2E;
  }
  barrier_lds();
  {
    const int row = sub * SUBN + c;
#pragma unroll
    for (int q = 0; q < 4; ++q) {
      const f32x4 o = *(const f32x4*)(lds + L_RING + (w ^ 2) * 8192 + q * 1024 + lane * 16);
      const u32x2 v = {pack2(mine[4 * q] + o[0], mine[4 * q + 1] + o[1]), pack2(mine[4 * q + 2] + o[2], mine[4 * q + 3] + o[3])};
      if (c < SUBN) *(u32x2*)(lds + L_AGG + 256 * row + 16 * ((4 * fbq + q) ^ (row & 15)) + 8 * h) = v;
    }
  }
  barrier_lds();
  STAMP(9);
#pragma unroll
  for (int ct = 0; ct < 3; ++ct) {
    const int row = min(16 * ct + n16, 2 * SUBN - 1);
#pragma unroll
    for (int s = 0; s < 4; ++s)
      z[ct] = mfma16(sub_frag(wu1, s), lds_frag(lds + L_AGG + 256 * row + 16 * ((4 * s + kq) ^ (row & 15))), z[ct]);
    float v[4];
#pragma unroll
    for (int i = 0; i < 4; ++i)
      v[i] = z[ct][i] * __builtin_amdgcn_rcpf(__builtin_fmaf(__builtin_amdgcn_exp2f(z[ct][i]), NEG_LOG2E, NEG_LOG2E));
    *(u32x2*)(lds + L_OUT + 256 * (16 * ct + n16) + 16 * ((2 * w + (kq >> 1)) ^ n16) + 8 * (kq & 1)) =
        (u32x2){pack2(v[0], v[1]), pack2(v[2], v[3])};
  }
  barrier_lds();
#pragma unroll
  for (int i = 0; i < 2; ++i) {
    const int k = tid + 512 * i, row = k >> 4, node = row * 256 + g;
    if (row < 2 * SUBN && node < NN)
      *(u32x4*)(out + (size_t)node * 128 + (k & 15) * 8) = *(const u32x4*)(lds + L_OUT + 256 * row + 16 * ((k & 15) ^ (row & 15)));
  }
  STAMP(10);
  STAMPW(11);
  STAMP_OUT(1, STW);
}

}

extern "C" void kernel_launch(void* const* d_in, const int* in_sizes, int n_in, void* d_out, int out_size, void* d_ws,
                              size_t ws_size, hipStream_t stream) {
  const unsigned short* x = (const unsigned short*)d_in[0];
  mpnn_prep<<<K1_GRID, 512, 0, stream>>>(x, (const u32x2*)d_in[1], (const int*)d_in[2], (const int*)d_in[3],
                                         (const unsigned short*)d_in[4], (const unsigned short*)d_in[5], (char*)d_ws);
  mpnn_edge<<<256, 512, 0, stream>>>(x, (const char*)d_ws, (unsigned short*)d_out);
}
```

```cpp
#include <hip/hip_runtime.h>
#include <hip/hip_bf16.h>
#include <stdint.h>

namespace {

typedef __attribute__((ext_vector_type(8))) __bf16 bf16x8;
typedef __attribute__((ext_vector_type(2))) __bf16 bf16x2;
typedef __attribute__((ext_vector_type(4))) short s16x4;
typedef __attribute__((ext_vector_type(16))) float f32x16;
typedef __attribute__((ext_vector_type(4))) float f32x4;
typedef __attribute__((ext_vector_type(2))) float f32x2;
typedef __attribute__((ext_vector_type(16))) unsigned u32x16;
typedef __attribute__((ext_vector_type(4))) unsigned u32x4;
typedef __attribute__((ext_vector_type(2))) unsigned u32x2;
struct __attribute__((packed, aligned(4))) Rec {
  unsigned a, e01, e23;
};
#define LDS_AS __attribute__((address_space(3)))
#define DI __device__ __forceinline__

constexpr int NN = 10000, NE = 160000;
constexpr int NPROD = 64, CHUNK = NE / NPROD;
constexpr int PBLK = (NN + 63) / 64;
constexpr int SUBN = 20;
constexpr float NEG_LOG2E = -1.4426950408889634f;

constexpr size_t WS_P = 0;
constexpr unsigned P_BYTES = PBLK * 64 * 256;
constexpr size_t WS_REC = WS_P + P_BYTES;
constexpr size_t WS_TAB = WS_REC + (size_t)NE * 12;
constexpr size_t WS_W2 = WS_TAB + NPROD * 2048;
constexpr size_t WS_WU = WS_W2 + 32768;
constexpr size_t WS_W3 = WS_WU + 65536;

#define STAMP_DECL
#define STAMP(i)
#define STAMPW(i)
#define STAMP_OUT(k, wv)

constexpr int K1_P0 = NPROD, K1_WT0 = K1_P0 + PBLK, K1_GRID = K1_WT0 + 13;

DI f32x16 mfma32(bf16x8 a, bf16x8 b, f32x16 c) { return __builtin_amdgcn_mfma_f32_32x32x16_bf16(a, b, c, 0, 0, 0); }
DI f32x4 mfma16(bf16x8 a, bf16x8 b, f32x4 c) { return __builtin_amdgcn_mfma_f32_16x16x32_bf16(a, b, c, 0, 0, 0); }
DI unsigned pack2(float a, float b) {
  bf16x2 v = {(__bf16)a, (__bf16)b};
  return __builtin_bit_cast(unsigned, v);
}
DI bf16x8 as_frag(u32x4 v) { return __builtin_bit_cast(bf16x8, v); }
DI bf16x8 sub_frag(const u32x16& v, int s) { return as_frag((u32x4){v[4 * s], v[4 * s + 1], v[4 * s + 2], v[4 * s + 3]}); }
DI bf16x8 lds_frag(const char* p) { return as_frag(*(const u32x4*)p); }
DI float bf2f(unsigned short u) { return __uint_as_float((unsigned)u << 16); }
DI unsigned swap23(unsigned v) { return (v & ~12u) | ((v & 4u) << 1) | ((v & 8u) >> 1); }
DI unsigned lds_addr(const char* p) { return (unsigned)(size_t)(const LDS_AS char*)p; }
DI unsigned wave_scan(unsigned v) {
  v += __builtin_amdgcn_update_dpp(0u, v, 0x111, 0xf, 0xf, false);
  v += __builtin_amdgcn_update_dpp(0u, v, 0x112, 0xf, 0xf, false);
  v += __builtin_amdgcn_update_dpp(0u, v, 0x114, 0xf, 0xf, false);
  v += __builtin_amdgcn_update_dpp(0u, v, 0x118, 0xf, 0xf, false);
  v += __builtin_amdgcn_update_dpp(0u, v, 0x142, 0xa, 0xf, false);
  v += __builtin_amdgcn_update_dpp(0u, v, 0x143, 0xc, 0xf, false);
  return v;
}
DI bf16x8 tr_frag(const char* lo, const char* hi) {
  s16x4 a = __builtin_amdgcn_ds_read_tr16_b64_v4i16((LDS_AS s16x4*)lo);
  s16x4 b = __builtin_amdgcn_ds_read_tr16_b64_v4i16((LDS_AS s16x4*)hi);
  return __builtin_bit_cast(bf16x8, __builtin_shufflevector(a, b, 0, 1, 2, 3, 4, 5, 6, 7));
}
DI u32x4 make_srd(const void* base, unsigned bytes) {
  const uint64_t pb = (uint64_t)base;
  u32x4 srd;
  srd[0] = __builtin_amdgcn_readfirstlane((unsigned)pb);
  srd[1] = __builtin_amdgcn_readfirstlane((unsigned)(pb >> 32));
  srd[2] = bytes;
  srd[3] = 0x00020000u;
  return srd;
}
DI void dma16(u32x4 srd, unsigned voff, unsigned dst) {
  unsigned keep;
  asm volatile("s_mov_b32 %0, m0\n\ts_mov_b32 m0, %3\n\ts_nop 2\n\tbuffer_load_dwordx4 %1, %2, 0 offen lds\n\ts_mov_b32 m0, %0"
               : "=&s"(keep)
               : "v"(voff), "s"(srd), "s"(dst)
               : "memory");
}
DI void st16(void* p, u32x4 v) { asm volatile("global_store_dwordx4 %0, %1, off sc1\n\ts_nop 1" :: "v"(p), "v"(v) : "memory"); }
DI void barrier_lds() {
  asm volatile("s_waitcnt lgkmcnt(0)\n\ts_barrier" ::: "memory");
}
DI void barrier_all() {
  asm volatile("s_waitcnt vmcnt(0) lgkmcnt(0)\n\ts_barrier" ::: "memory");
}

constexpr int K1_LDS = 83968;

__global__ __launch_bounds__(512) void mpnn_prep(const unsigned short* __restrict__ x, const u32x2* __restrict__ eattr,
                                                 const int* __restrict__ src, const int* __restrict__ dst,
                                                 const unsigned short* __restrict__ wmsg,
                                                 const unsigned short* __restrict__ wupd, char* __restrict__ ws) {
  __shared__ __attribute__((aligned(16))) char lds[K1_LDS];
  const int tid = threadIdx.x, lane = tid & 63, w = __builtin_amdgcn_readfirstlane(tid >> 6);
  const int b = blockIdx.x;
  STAMP_DECL
  STAMP(0);
  if (b < K1_P0) {
    const int e0 = b * CHUNK;
    unsigned* hist = (unsigned*)lds;
    unsigned short* start = (unsigned short*)(lds + 2048 + w * 1024);
    unsigned* stage = (unsigned*)(lds + 10240);
    int dv[5], sv[5];
    u32x2 ev[5];
#pragma unroll
    for (int i = 0; i < 5; ++i) dv[i] = dst[e0 + min(tid + 512 * i, CHUNK - 1)];
#pragma unroll
    for (int i = 0; i < 5; ++i) {
      const int k = min(tid + 512 * i, CHUNK - 1);
      sv[i] = src[e0 + k];
      ev[i] = eattr[e0 + k];
    }
    hist[tid] = 0;
    barrier_lds();
    unsigned key[5], rank[5];
#pragma unroll
    for (int i = 0; i < 5; ++i) {
      const unsigned d = dv[i], g = d & 255u, j = d >> 8, sub = j >= SUBN;
      key[i] = (((g & 7u) << 5) + (g >> 3)) * 2 + sub;
      dv[i] = swap23(j - SUBN * sub);
      rank[i] = 0;
      if (tid + 512 * i < CHUNK) rank[i] = atomicAdd(&hist[key[i]], 1u);
    }
    STAMP(1);
    barrier_lds();
    const u32x4 c0 = *(const u32x4*)(hist + 8 * lane), c1 = *(const u32x4*)(hist + 8 * lane + 4);
    const unsigned tot = c0[0] + c0[1] + c0[2] + c0[3] + c1[0] + c1[1] + c1[2] + c1[3];
    unsigned s[9];
    s[0] = wave_scan(tot) - tot;
#pragma unroll
    for (int i = 0; i < 4; ++i) s[i + 1] = s[i] + c0[i];
#pragma unroll
    for (int i = 0; i < 4; ++i) s[i + 5] = s[i + 4] + c1[i];
    const u32x4 sp = {s[0] | (s[1] << 16), s[2] | (s[3] << 16), s[4] | (s[5] << 16), s[6] | (s[7] << 16)};
    *(u32x4*)(start + 8 * lane) = sp;
    if (w < 2) {
      const u32x4 t = w ? (u32x4){sp[2], s[6], sp[3], s[8]} : (u32x4){sp[0], s[2], sp[1], s[4]};
      st16(ws + WS_TAB + b * 2048 + lane * 32 + w * 16, t);
    }
    __builtin_amdgcn_wave_barrier();
#pragma unroll
    for (int i = 0; i < 5; ++i) {
      if (tid + 512 * i < CHUNK) {
        unsigned* r = stage + 3 * (start[key[i]] + rank[i]);
        r[0] = ((unsigned)sv[i] << 8) | dv[i];
        r[1] = ev[i][0];
        r[2] = ev[i][1];
      }
    }
    STAMP(2);
    barrier_lds();
    u32x4* go = (u32x4*)(ws + WS_REC + (size_t)e0 * 12);
#pragma unroll
    for (int i = 0; i < 4; ++i) {
      const int k = tid + 512 * i;
      if (k < CHUNK * 12 / 16) st16(go + k, ((const u32x4*)stage)[k]);
    }
    STAMP(3);
    STAMPW(4);
  } else if (b < K1_WT0) {
    const int n0 = (b - K1_P0) * 64;
    char* wimg = lds;
    char* ximg = lds + 32768;
    char* oimg = lds + 49152;
    const u32x4 xs = make_srd(x, NN * 256), wsd = make_srd(wmsg, 128 * 256);
    const unsigned r4 = lane >> 4, c16 = lane & 15;
#pragma unroll
    for (int i = 0; i < 2; ++i) {
      const unsigned row = (w * 2 + i) * 4 + r4;
      dma16(xs, (n0 + row) * 256 + ((c16 ^ (row & 15)) << 4), lds_addr(ximg) + (w * 2 + i) * 1024);
    }
#pragma unroll
    for (int i = 0; i < 4; ++i) {
      const unsigned row = (w * 4 + i) * 4 + r4;
      dma16(wsd, row * 256 + ((c16 ^ (((row & 3) << 2) | ((row >> 2) & 3))) << 4), lds_addr(wimg) + (w * 4 + i) * 1024);
    }
    barrier_all();
    STAMP(5);
    const int c = lane & 31, h = lane >> 5, fb = w & 3, xrow = (w >> 2) * 32 + c;
    const int blk = (lane >> 4) & 1, q = (lane & 15) >> 2, p = lane & 3, ch = 4 * fb + 2 * blk + (p >> 1);
    f32x16 acc = {};
#pragma unroll
    for (int s = 0; s < 8; ++s) {
      const int r0 = 16 * s + 8 * h + q, r1 = r0 + 4;
      const bf16x8 a = tr_frag(wimg + 256 * r0 + 16 * (ch ^ (((r0 & 3) << 2) | ((r0 >> 2) & 3))) + 8 * (p & 1),
                               wimg + 256 * r1 + 16 * (ch ^ (((r1 & 3) << 2) | ((r1 >> 2) & 3))) + 8 * (p & 1));
      acc = mfma32(a, lds_frag(ximg + 256 * xrow + 16 * ((2 * s + h) ^ (xrow & 15))), acc);
    }
    u32x4 o0, o1;
#pragma unroll
    for (int i = 0; i < 4; ++i) {
      o0[i] = pack2(acc[2 * i] * NEG_LOG2E, acc[2 * i + 1] * NEG_LOG2E);
      o1[i] = pack2(acc[8 + 2 * i] * NEG_LOG2E, acc[9 + 2 * i] * NEG_LOG2E);
    }
    *(u32x4*)(oimg + 256 * xrow + 16 * ((4 * fb + 2 * h) ^ (xrow & 15))) = o0;
    *(u32x4*)(oimg + 256 * xrow + 16 * ((4 * fb + 2 * h + 1) ^ (xrow & 15))) = o1;
    STAMP(6);
    barrier_lds();
#pragma unroll
    for (int i = 0; i < 2; ++i) {
      const int k = tid + 512 * i, row = k >> 4;
      st16(ws + WS_P + (size_t)n0 * 256 + k * 16, *(const u32x4*)(oimg + 256 * row + 16 * ((k & 15) ^ (row & 15))));
    }
    STAMP(7);
    STAMPW(8);
  } else if (b < K1_GRID - 1) {
    const int t = (b - K1_WT0) * 512 + tid;
    const unsigned short* wp;
    if (t < 2048) wp = wmsg + (128 + 16 * ((t >> 6) & 7) + 8 * (lane >> 5)) * 128 + (t >> 9) * 32 + (lane & 31);
    else wp = wupd + (32 * ((t >> 6) & 7) + 8 * (lane >> 4)) * 128 + ((t - 2048) >> 9) * 16 + (lane & 15);
    unsigned v[8];
#pragma unroll
    for (int j = 0; j < 8; ++j) v[j] = wp[j * 128];
    u32x4 o = {v[0] | (v[1] << 16), v[2] | (v[3] << 16), v[4] | (v[5] << 16), v[6] | (v[7] << 16)};
    st16(ws + WS_W2 + (size_t)t * 16, o);
    STAMPW(9);
  } else if (tid < 128) {
    float hi[4], lo[4];
#pragma unroll
    for (int k = 0; k < 4; ++k) {
      const float v = bf2f(wmsg[(256 + k) * 128 + tid]) * NEG_LOG2E;
      hi[k] = (float)(__bf16)v;
      lo[k] = v - hi[k];
    }
    u32x4 o = {pack2(lo[0], lo[1]), pack2(lo[2], lo[3]), pack2(hi[0], hi[1]), pack2(hi[2], hi[3])};
    st16(ws + WS_W3 + tid * 16, o);
  }
  STAMP_OUT(0, 0);
}

constexpr int CAP = 416;
constexpr int L_S = 0;
constexpr int L_LIST = L_S + (2 * CAP + 32) * 64;
constexpr int L_ZEND = L_LIST + (2 * CAP + 32) * 4;
constexpr int L_WSUM = L_ZEND;
constexpr int L_X = L_WSUM + 128;
constexpr int L_RING = L_X + 48 * 256;
constexpr int L_TOTAL = L_RING + 8 * 8192;
constexpr int L_AGG = L_S, L_OUT = L_S + 48 * 256;
constexpr unsigned OOB = 0x40000000u;
constexpr int NREC = 5;

DI void edge_tiles(f32x16& agg0, f32x16& agg1, u32x16& wu0, u32x16& wu1, bf16x8 q00, bf16x8 q01, bf16x8 q10, bf16x8 q11,
                   bf16x8 id0, bf16x8 id1, unsigned pa0, unsigned sa0, unsigned sa1, unsigned st0, unsigned st1,
                   unsigned lst, unsigned s0, unsigned wuo, u32x4 srd, const char* wub, unsigned ring, int nf, int m) {
  int k = nf + (m > 0), t;
  unsigned keep;
  asm volatile(
      "s_waitcnt lgkmcnt(0)\n\t"
      "s_mov_b32 %[keep], m0\n\t"
      "ds_read_b32 v216, %[lst] offset:0\n\t"
      "ds_read_b32 v218, %[lst] offset:32\n\t"
      "ds_read_b32 v220, %[lst] offset:64\n\t"
      "ds_read_b32 v222, %[lst] offset:96\n\t"
      "ds_read_b32 v160, %[lst] offset:128\n\t"
      "ds_read_b32 v162, %[lst] offset:160\n\t"
      "ds_read_b32 v164, %[lst] offset:192\n\t"
      "ds_read_b32 v166, %[lst] offset:224\n\t"
      "ds_read_b32 v168, %[lst] offset:256\n\t"
      "ds_read_b32 v170, %[lst] offset:288\n\t"
      "ds_read_b32 v172, %[lst] offset:320\n\t"
      "ds_read_b32 v174, %[lst] offset:352\n\t"
      "v_mov_b32 v217, %[s0]\n\t"
      "v_mov_b32 v219, %[s1]\n\t"
      "v_mov_b32 v221, %[s0]\n\t"
      "v_mov_b32 v223, %[s1]\n\t"
      "v_mov_b32 v161, %[s0]\n\t"
      "v_mov_b32 v163, %[s1]\n\t"
      "v_mov_b32 v165, %[s0]\n\t"
      "v_mov_b32 v167, %[s1]\n\t"
      "v_mov_b32 v169, %[s0]\n\t"
      "v_mov_b32 v171, %[s1]\n\t"
      "v_mov_b32 v173, %[s0]\n\t"
      "v_mov_b32 v175, %[s1]\n\t"
      "s_waitcnt lgkmcnt(8)\n\t"
      "s_cmp_lt_i32 %[k], 1\n\t"
      "s_cbranch_scc1 Lnd1_%=\n\t"
      "s_add_u32 m0, %[ring], 0\n\t"
      "s_nop 0\n\t"
      "buffer_load_dwordx4 v[216:217], %[srd], 0 idxen offen lds\n\t"
      "s_add_u32 m0, %[ring], 1024\n\t"
      "s_nop 0\n\t"
      "buffer_load_dwordx4 v[218:219], %[srd], 0 idxen offen lds\n\t"
      "s_add_u32 m0, %[ring], 2048\n\t"
      "s_nop 0\n\t"
      "buffer_load_dwordx4 v[220:221], %[srd], 0 idxen offen lds\n\t"
      "s_add_u32 m0, %[ring], 3072\n\t"
      "s_nop 0\n\t"
      "buffer_load_dwordx4 v[222:223], %[srd], 0 idxen offen lds\n\t"
      "s_branch Lge1_%=\n\t"
      "Lnd1_%=:\n\t"
      "s_cmp_lg_i32 %[k], 0\n\t"
      "s_cbranch_scc1 Lge1_%=\n\t"
      "global_load_dwordx4 v[224:227], %[wuo], %[wub] offset:-4096\n\t"
      "global_load_dwordx4 v[228:231], %[wuo], %[wub] offset:-3072\n\t"
      "global_load_dwordx4 v[232:235], %[wuo], %[wub] offset:-2048\n\t"
      "global_load_dwordx4 v[236:239], %[wuo], %[wub] offset:-1024\n\t"
      "global_load_dwordx4 v[240:243], %[wuo], %[wub] offset:0\n\t"
      "global_load_dwordx4 v[244:247], %[wuo], %[wub] offset:1024\n\t"
      "global_load_dwordx4 v[248:251], %[wuo], %[wub] offset:2048\n\t"
      "global_load_dwordx4 v[252:255], %[wuo], %[wub] offset:3072\n\t"
      "Lge1_%=:\n\t"
      "s_sub_i32 %[k], %[k], 1\n\t"
      "s_waitcnt lgkmcnt(4)\n\t"
      "s_cmp_lt_i32 %[k], 1\n\t"
      "s_cbranch_scc1 Lnd2_%=\n\t"
      "s_add_u32 m0, %[ring], 4096\n\t"
      "s_nop 0\n\t"
      "buffer_load_dwordx4 v[160:161], %[srd], 0 idxen offen lds\n\t"
      "s_add_u32 m0, %[ring], 5120\n\t"
      "s_nop 0\n\t"
      "buffer_load_dwordx4 v[162:163], %[srd], 0 idxen offen lds\n\t"
      "s_add_u32 m0, %[ring], 6144\n\t"
      "s_nop 0\n\t"
      "buffer_load_dwordx4 v[164:165], %[srd], 0 idxen offen lds\n\t"
      "s_add_u32 m0, %[ring], 7168\n\t"
      "s_nop 0\n\t"
      "buffer_load_dwordx4 v[166:167], %[srd], 0 idxen offen lds\n\t"
      "s_branch Lge2_%=\n\t"
      "Lnd2_%=:\n\t"
      "s_cmp_lg_i32 %[k], 0\n\t"
      "s_cbranch_scc1 Lge2_%=\n\t"
      "global_load_dwordx4 v[224:227], %[wuo], %[wub] offset:-4096\n\t"
      "global_load_dwordx4 v[228:231], %[wuo], %[wub] offset:-3072\n\t"
      "global_load_dwordx4 v[232:235], %[wuo], %[wub] offset:-2048\n\t"
      "global_load_dwordx4 v[236:239], %[wuo], %[wub] offset:-1024\n\t"
      "global_load_dwordx4 v[240:243], %[wuo], %[wub] offset:0\n\t"
      "global_load_dwordx4 v[244:247], %[wuo], %[wub] offset:1024\n\t"
      "global_load_dwordx4 v[248:251], %[wuo], %[wub] offset:2048\n\t"
      "global_load_dwordx4 v[252:255], %[wuo], %[wub] offset:3072\n\t"
      "Lge2_%=:\n\t"
      "s_sub_i32 %[k], %[k], 1\n\t"
      "v_mov_b32 v176, 0\n\t"
      "v_mov_b32 v177, 0\n\t"
      "v_mov_b32 v178, 0\n\t"
      "v_mov_b32 v179, 0\n\t"
      "v_mov_b32 v180, 0\n\t"
      "v_mov_b32 v181, 0\n\t"
      "v_mov_b32 v182, 0\n\t"
      "v_mov_b32 v183, 0\n\t"
      "v_mov_b32 v208, 0\n\t"
      "v_mov_b32 v209, 0\n\t"
      "v_mov_b32 v210, 0\n\t"
      "v_mov_b32 v211, 0\n\t"
      "v_mov_b32 v212, 0\n\t"
      "v_mov_b32 v213, 0\n\t"
      "v_mov_b32 v214, 0\n\t"
      "v_mov_b32 v215, 0\n\t"
      "s_add_u32 %[t], %[nf], %[m]\n\t"
      "s_cmp_eq_u32 %[t], 0\n\t"
      "s_cbranch_scc1 Ldone_%=\n\t"
      "ds_read_b128 v[200:203], %[sa0] offset:0\n\t"
      "ds_read_b128 v[204:207], %[sa1] offset:0\n\t"
      "s_cmp_lt_i32 %[k], 0\n\t"
      "s_cbranch_scc1 Lw83_%=\n\t"
      "s_waitcnt vmcnt(4)\n\t"
      "Lw83_%=:\n\t"
      "s_waitcnt vmcnt(8)\n\t"
      "ds_read_b128 v[184:187], %[pa0] offset:0\n\t"
      "ds_read_b128 v[188:191], %[pa1] offset:0\n\t"
      "ds_read_b128 v[192:195], %[pa2] offset:0\n\t"
      "ds_read_b128 v[196:199], %[pa3] offset:0\n\t"
      "s_waitcnt lgkmcnt(4)\n\t"
      "v_mfma_f32_32x32x16_bf16 v[128:143], v[200:203], %[q00], 0\n\t"
      "v_mfma_f32_32x32x16_bf16 v[128:143], v[204:207], %[q01], v[128:143]\n\t"
      "s_waitcnt lgkmcnt(0)\n\t"
      "s_cmp_lt_i32 %[k], 1\n\t"
      "s_cbranch_scc1 Lnd4_%=\n\t"
      "s_add_u32 m0, %[ring], 0\n\t"
      "s_nop 0\n\t"
      "buffer_load_dwordx4 v[168:169], %[srd], 0 idxen offen lds\n\t"
      "s_add_u32 m0, %[ring], 1024\n\t"
      "s_nop 0\n\t"
      "buffer_load_dwordx4 v[170:171], %[srd], 0 idxen offen lds\n\t"
      "s_add_u32 m0, %[ring], 2048\n\t"
      "s_nop 0\n\t"
      "buffer_load_dwordx4 v[172:173], %[srd], 0 idxen offen lds\n\t"
      "s_add_u32 m0, %[ring], 3072\n\t"
      "s_nop 0\n\t"
      "buffer_load_dwordx4 v[174:175], %[srd], 0 idxen offen lds\n\t"
      "s_branch Lge4_%=\n\t"
      "Lnd4_%=:\n\t"
      "s_cmp_lg_i32 %[k], 0\n\t"
      "s_cbranch_scc1 Lge4_%=\n\t"
      "global_load_dwordx4 v[224:227], %[wuo], %[wub] offset:-4096\n\t"
      "global_load_dwordx4 v[228:231], %[wuo], %[wub] offset:-3072\n\t"
      "global_load_dwordx4 v[232:235], %[wuo], %[wub] offset:-2048\n\t"
      "global_load_dwordx4 v[236:239], %[wuo], %[wub] offset:-1024\n\t"
      "global_load_dwordx4 v[240:243], %[wuo], %[wub] offset:0\n\t"
      "global_load_dwordx4 v[244:247], %[wuo], %[wub] offset:1024\n\t"
      "global_load_dwordx4 v[248:251], %[wuo], %[wub] offset:2048\n\t"
      "global_load_dwordx4 v[252:255], %[wuo], %[wub] offset:3072\n\t"
      "Lge4_%=:\n\t"
      "s_sub_i32 %[k], %[k], 1\n\t"
      "v_mfma_f32_32x32x16_bf16 v[128:143], v[184:187], %[id0], v[128:143]\n\t"
      "v_mfma_f32_32x32x16_bf16 v[128:143], v[188:191], %[id1], v[128:143]\n\t"
      "s_mov_b32 %[t], %[nf]\n\t"
      "s_cmp_eq_u32 %[t], 0\n\t"
      "s_nop 9\n\t"
      "s_cbranch_scc1 Ltail_%=\n\t"
      "Lloop_%=:\n\t"
      "v_exp_f32 v160, v128\n\t"
      "v_mfma_f32_32x32x16_bf16 v[144:159], v[200:203], %[q10], 0\n\t"
      "v_exp_f32 v161, v129\n\t"
      "v_exp_f32 v162, v130\n\t"
      "v_exp_f32 v163, v131\n\t"
      "v_exp_f32 v164, v132\n\t"
      "v_mfma_f32_32x32x16_bf16 v[144:159], v[204:207], %[q11], v[144:159]\n\t"
      "v_exp_f32 v165, v133\n\t"
      "s_cmp_lt_i32 %[k], 0\n\t"
      "s_cbranch_scc1 Lw85_%=\n\t"
      "s_waitcnt vmcnt(4)\n\t"
      "Lw85_%=:\n\t"
      "s_waitcnt vmcnt(8)\n\t"
      "ds_read_b128 v[200:203], %[sa0] offset:2048\n\t"
      "ds_read_b128 v[204:207], %[sa1] offset:2048\n\t"
      "ds_read_b128 v[184:187], %[pa0] offset:4096\n\t"
      "ds_read_b128 v[188:191], %[pa1] offset:4096\n\t"
      "v_exp_f32 v166, v134\n\t"
      "v_exp_f32 v167, v135\n\t"
      "v_exp_f32 v168, v136\n\t"
      "v_mfma_f32_32x32x16_bf16 v[144:159], v[192:195], %[id0], v[144:159]\n\t"
      "v_exp_f32 v169, v137\n\t"
      "v_exp_f32 v170, v138\n\t"
      "v_exp_f32 v171, v139\n\t"
      "v_exp_f32 v172, v140\n\t"
      "v_exp_f32 v173, v141\n\t"
      "v_exp_f32 v174, v142\n\t"
      "v_exp_f32 v175, v143\n\t"
      "v_pk_add_f32 v[160:161], v[160:161], 1.0 op_sel_hi:[1,0]\n\t"
      "v_pk_add_f32 v[162:163], v[162:163], 1.0 op_sel_hi:[1,0]\n\t"
      "v_pk_add_f32 v[164:165], v[164:165], 1.0 op_sel_hi:[1,0]\n\t"
      "v_pk_add_f32 v[166:167], v[166:167], 1.0 op_sel_hi:[1,0]\n\t"
      "v_pk_add_f32 v[168:169], v[168:169], 1.0 op_sel_hi:[1,0]\n\t"
      "v_pk_add_f32 v[170:171], v[170:171], 1.0 op_sel_hi:[1,0]\n\t"
      "v_pk_add_f32 v[172:173], v[172:173], 1.0 op_sel_hi:[1,0]\n\t"
      "v_pk_add_f32 v[174:175], v[174:175], 1.0 op_sel_hi:[1,0]\n\t"
      "v_rcp_f32 v160, v160\n\t"
      "v_mfma_f32_32x32x16_bf16 v[144:159], v[196:199], %[id1], v[144:159]\n\t"
      "v_rcp_f32 v161, v161\n\t"
      "ds_read_b128 v[192:195], %[pa2] offset:4096\n\t"
      "ds_read_b128 v[196:199], %[pa3] offset:4096\n\t"
      "ds_read_b32 v216, %[lst] offset:384\n\t"
      "ds_read_b32 v218, %[lst] offset:416\n\t"
      "ds_read_b32 v220, %[lst] offset:448\n\t"
      "ds_read_b32 v222, %[lst] offset:480\n\t"
      "v_rcp_f32 v162, v162\n\t"
      "v_rcp_f32 v163, v163\n\t"
      "v_rcp_f32 v164, v164\n\t"
      "v_mfma_f32_32x32x16_bf16 %[ag1], v[176:179], v[208:211], %[ag1]\n\t"
      "v_rcp_f32 v165, v165\n\t"
      "v_rcp_f32 v166, v166\n\t"
      "v_rcp_f32 v167, v167\n\t"
      "v_rcp_f32 v168, v168\n\t"
      "v_mfma_f32_32x32x16_bf16 %[ag1], v[180:183], v[212:215], %[ag1]\n\t"
      "v_rcp_f32 v169, v169\n\t"
      "ds_read_b64_tr_b16 v[208:209], %[st0] offset:0\n\t"
      "ds_read_b64_tr_b16 v[210:211], %[st1] offset:0\n\t"
      "ds_read_b64_tr_b16 v[212:213], %[st0] offset:1024\n\t"
      "ds_read_b64_tr_b16 v[214:215], %[st1] offset:1024\n\t"
      "v_rcp_f32 v170, v170\n\t"
      "v_rcp_f32 v171, v171\n\t"
      "v_rcp_f32 v172, v172\n\t"
      "v_rcp_f32 v173, v173\n\t"
      "v_rcp_f32 v174, v174\n\t"
      "v_rcp_f32 v175, v175\n\t"
      "v_pk_mul_f32 v[160:161], v[128:129], v[160:161]\n\t"
      "v_pk_mul_f32 v[162:163], v[130:131], v[162:163]\n\t"
      "v_pk_mul_f32 v[164:165], v[132:133], v[164:165]\n\t"
      "v_pk_mul_f32 v[166:167], v[134:135], v[166:167]\n\t"
      "v_pk_mul_f32 v[168:169], v[136:137], v[168:169]\n\t"
      "v_pk_mul_f32 v[170:171], v[138:139], v[170:171]\n\t"
      "v_pk_mul_f32 v[172:173], v[140:141], v[172:173]\n\t"
      "v_pk_mul_f32 v[174:175], v[142:143], v[174:175]\n\t"
      "v_cvt_pk_bf16_f32 v176, v160, v161\n\t"
      "v_cvt_pk_bf16_f32 v177, v162, v163\n\t"
      "v_cvt_pk_bf16_f32 v178, v164, v165\n\t"
      "v_cvt_pk_bf16_f32 v179, v166, v167\n\t"
      "v_cvt_pk_bf16_f32 v180, v168, v169\n\t"
      "v_cvt_pk_bf16_f32 v181, v170, v171\n\t"
      "v_cvt_pk_bf16_f32 v182, v172, v173\n\t"
      "v_cvt_pk_bf16_f32 v183, v174, v175\n\t"
      "s_waitcnt lgkmcnt(4)\n\t"
      "s_cmp_lt_i32 %[k], 1\n\t"
      "s_cbranch_scc1 Lnd6_%=\n\t"
      "s_add_u32 m0, %[ring], 4096\n\t"
      "s_nop 0\n\t"
      "buffer_load_dwordx4 v[216:217], %[srd], 0 idxen offen lds\n\t"
      "s_add_u32 m0, %[ring], 5120\n\t"
      "s_nop 0\n\t"
      "buffer_load_dwordx4 v[218:219], %[srd], 0 idxen offen lds\n\t"
      "s_add_u32 m0, %[ring], 6144\n\t"
      "s_nop 0\n\t"
      "buffer_load_dwordx4 v[220:221], %[srd], 0 idxen offen lds\n\t"
      "s_add_u32 m0, %[ring], 7168\n\t"
      "s_nop 0\n\t"
      "buffer_load_dwordx4 v[222:223], %[srd], 0 idxen offen lds\n\t"
      "s_branch Lge6_%=\n\t"
      "Lnd6_%=:\n\t"
      "s_cmp_lg_i32 %[k], 0\n\t"
      "s_cbranch_scc1 Lge6_%=\n\t"
      "global_load_dwordx4 v[224:227], %[wuo], %[wub] offset:-4096\n\t"
      "global_load_dwordx4 v[228:231], %[wuo], %[wub] offset:-3072\n\t"
      "global_load_dwordx4 v[232:235], %[wuo], %[wub] offset:-2048\n\t"
      "global_load_dwordx4 v[236:239], %[wuo], %[wub] offset:-1024\n\t"
      "global_load_dwordx4 v[240:243], %[wuo], %[wub] offset:0\n\t"
      "global_load_dwordx4 v[244:247], %[wuo], %[wub] offset:1024\n\t"
      "global_load_dwordx4 v[248:251], %[wuo], %[wub] offset:2048\n\t"
      "global_load_dwordx4 v[252:255], %[wuo], %[wub] offset:3072\n\t"
      "Lge6_%=:\n\t"
      "s_sub_i32 %[k], %[k], 1\n\t"
      "v_exp_f32 v160, v144\n\t"
      "v_mfma_f32_32x32x16_bf16 v[128:143], v[200:203], %[q00], 0\n\t"
      "v_exp_f32 v161, v145\n\t"
      "v_exp_f32 v162, v146\n\t"
      "v_exp_f32 v163, v147\n\t"
      "v_exp_f32 v164, v148\n\t"
      "v_mfma_f32_32x32x16_bf16 v[128:143], v[204:207], %[q01], v[128:143]\n\t"
      "v_exp_f32 v165, v149\n\t"
      "v_exp_f32 v166, v150\n\t"
      "v_exp_f32 v167, v151\n\t"
      "v_exp_f32 v168, v152\n\t"
      "v_mfma_f32_32x32x16_bf16 v[128:143], v[184:187], %[id0], v[128:143]\n\t"
      "v_exp_f32 v169, v153\n\t"
      "v_exp_f32 v170, v154\n\t"
      "v_exp_f32 v171, v155\n\t"
      "v_exp_f32 v172, v156\n\t"
      "v_exp_f32 v173, v157\n\t"
      "v_exp_f32 v174, v158\n\t"
      "v_exp_f32 v175, v159\n\t"
      "v_pk_add_f32 v[160:161], v[160:161], 1.0 op_sel_hi:[1,0]\n\t"
      "v_pk_add_f32 v[162:163], v[162:163], 1.0 op_sel_hi:[1,0]\n\t"
      "v_pk_add_f32 v[164:165], v[164:165], 1.0 op_sel_hi:[1,0]\n\t"
      "v_pk_add_f32 v[166:167], v[166:167], 1.0 op_sel_hi:[1,0]\n\t"
      "v_pk_add_f32 v[168:169], v[168:169], 1.0 op_sel_hi:[1,0]\n\t"
      "v_pk_add_f32 v[170:171], v[170:171], 1.0 op_sel_hi:[1,0]\n\t"
      "v_pk_add_f32 v[172:173], v[172:173], 1.0 op_sel_hi:[1,0]\n\t"
      "v_pk_add_f32 v[174:175], v[174:175], 1.0 op_sel_hi:[1,0]\n\t"
      "v_rcp_f32 v160, v160\n\t"
      "v_mfma_f32_32x32x16_bf16 v[128:143], v[188:191], %[id1], v[128:143]\n\t"
      "v_rcp_f32 v161, v161\n\t"
      "s_waitcnt lgkmcnt(0)\n\t"
      "v_rcp_f32 v162, v162\n\t"
      "v_rcp_f32 v163, v163\n\t"
      "v_rcp_f32 v164, v164\n\t"
      "v_mfma_f32_32x32x16_bf16 %[ag0], v[176:179], v[208:211], %[ag0]\n\t"
      "v_rcp_f32 v165, v165\n\t"
      "v_rcp_f32 v166, v166\n\t"
      "v_rcp_f32 v167, v167\n\t"
      "v_rcp_f32 v168, v168\n\t"
      "v_mfma_f32_32x32x16_bf16 %[ag0], v[180:183], v[212:215], %[ag0]\n\t"
      "v_rcp_f32 v169, v169\n\t"
      "v_rcp_f32 v170, v170\n\t"
      "v_rcp_f32 v171, v171\n\t"
      "v_rcp_f32 v172, v172\n\t"
      "v_rcp_f32 v173, v173\n\t"
      "v_rcp_f32 v174, v174\n\t"
      "v_rcp_f32 v175, v175\n\t"
      "v_pk_mul_f32 v[160:161], v[144:145], v[160:161]\n\t"
      "v_pk_mul_f32 v[162:163], v[146:147], v[162:163]\n\t"
      "v_pk_mul_f32 v[164:165], v[148:149], v[164:165]\n\t"
      "v_pk_mul_f32 v[166:167], v[150:151], v[166:167]\n\t"
      "v_pk_mul_f32 v[168:169], v[152:153], v[168:169]\n\t"
      "v_pk_mul_f32 v[170:171], v[154:155], v[170:171]\n\t"
      "v_pk_mul_f32 v[172:173], v[156:157], v[172:173]\n\t"
      "v_pk_mul_f32 v[174:175], v[158:159], v[174:175]\n\t"
      "v_cvt_pk_bf16_f32 v176, v160, v161\n\t"
      "v_cvt_pk_bf16_f32 v177, v162, v163\n\t"
      "v_cvt_pk_bf16_f32 v178, v164, v165\n\t"
      "v_cvt_pk_bf16_f32 v179, v166, v167\n\t"
      "v_cvt_pk_bf16_f32 v180, v168, v169\n\t"
      "v_cvt_pk_bf16_f32 v181, v170, v171\n\t"
      "v_cvt_pk_bf16_f32 v182, v172, v173\n\t"
      "v_cvt_pk_bf16_f32 v183, v174, v175\n\t"
      "s_sub_u32 %[t], %[t], 1\n\t"
      "s_cmp_eq_u32 %[t], 0\n\t"
      "s_cbranch_scc1 Lexit_even_%=\n\t"
      "v_exp_f32 v160, v128\n\t"
      "v_mfma_f32_32x32x16_bf16 v[144:159], v[200:203], %[q10], 0\n\t"
      "v_exp_f32 v161, v129\n\t"
      "v_exp_f32 v162, v130\n\t"
      "v_exp_f32 v163, v131\n\t"
      "v_exp_f32 v164, v132\n\t"
      "v_mfma_f32_32x32x16_bf16 v[144:159], v[204:207], %[q11], v[144:159]\n\t"
      "v_exp_f32 v165, v133\n\t"
      "s_cmp_lt_i32 %[k], 0\n\t"
      "s_cbranch_scc1 Lw87_%=\n\t"
      "s_waitcnt vmcnt(4)\n\t"
      "Lw87_%=:\n\t"
      "s_waitcnt vmcnt(8)\n\t"
      "ds_read_b128 v[200:203], %[sa0] offset:4096\n\t"
      "ds_read_b128 v[204:207], %[sa1] offset:4096\n\t"
      "ds_read_b128 v[184:187], %[pa0] offset:0\n\t"
      "ds_read_b128 v[188:191], %[pa1] offset:0\n\t"
      "v_exp_f32 v166, v134\n\t"
      "v_exp_f32 v167, v135\n\t"
      "v_exp_f32 v168, v136\n\t"
      "v_mfma_f32_32x32x16_bf16 v[144:159], v[192:195], %[id0], v[144:159]\n\t"
      "v_exp_f32 v169, v137\n\t"
      "v_exp_f32 v170, v138\n\t"
      "v_exp_f32 v171, v139\n\t"
      "v_exp_f32 v172, v140\n\t"
      "v_exp_f32 v173, v141\n\t"
      "v_exp_f32 v174, v142\n\t"
      "v_exp_f32 v175, v143\n\t"
      "v_pk_add_f32 v[160:161], v[160:161], 1.0 op_sel_hi:[1,0]\n\t"
      "v_pk_add_f32 v[162:163], v[162:163], 1.0 op_sel_hi:[1,0]\n\t"
      "v_pk_add_f32 v[164:165], v[164:165], 1.0 op_sel_hi:[1,0]\n\t"
      "v_pk_add_f32 v[166:167], v[166:167], 1.0 op_sel_hi:[1,0]\n\t"
      "v_pk_add_f32 v[168:169], v[168:169], 1.0 op_sel_hi:[1,0]\n\t"
      "v_pk_add_f32 v[170:171], v[170:171], 1.0 op_sel_hi:[1,0]\n\t"
      "v_pk_add_f32 v[172:173], v[172:173], 1.0 op_sel_hi:[1,0]\n\t"
      "v_pk_add_f32 v[174:175], v[174:175], 1.0 op_sel_hi:[1,0]\n\t"
      "v_rcp_f32 v160, v160\n\t"
      "v_mfma_f32_32x32x16_bf16 v[144:159], v[196:199], %[id1], v[144:159]\n\t"
      "v_rcp_f32 v161, v161\n\t"
      "ds_read_b128 v[192:195], %[pa2] offset:0\n\t"
      "ds_read_b128 v[196:199], %[pa3] offset:0\n\t"
      "ds_read_b32 v216, %[lst] offset:512\n\t"
      "ds_read_b32 v218, %[lst] offset:544\n\t"
      "ds_read_b32 v220, %[lst] offset:576\n\t"
      "ds_read_b32 v222, %[lst] offset:608\n\t"
      "v_rcp_f32 v162, v162\n\t"
      "v_rcp_f32 v163, v163\n\t"
      "v_rcp_f32 v164, v164\n\t"
      "v_mfma_f32_32x32x16_bf16 %[ag1], v[176:179], v[208:211], %[ag1]\n\t"
      "v_rcp_f32 v165, v165\n\t"
      "v_rcp_f32 v166, v166\n\t"
      "v_rcp_f32 v167, v167\n\t"
      "v_rcp_f32 v168, v168\n\t"
      "v_mfma_f32_32x32x16_bf16 %[ag1], v[180:183], v[212:215], %[ag1]\n\t"
      "v_rcp_f32 v169, v169\n\t"
      "ds_read_b64_tr_b16 v[208:209], %[st0] offset:2048\n\t"
      "ds_read_b64_tr_b16 v[210:211], %[st1] offset:2048\n\t"
      "ds_read_b64_tr_b16 v[212:213], %[st0] offset:3072\n\t"
      "ds_read_b64_tr_b16 v[214:215], %[st1] offset:3072\n\t"
      "v_rcp_f32 v170, v170\n\t"
      "v_rcp_f32 v171, v171\n\t"
      "v_rcp_f32 v172, v172\n\t"
      "v_rcp_f32 v173, v173\n\t"
      "v_rcp_f32 v174, v174\n\t"
      "v_rcp_f32 v175, v175\n\t"
      "v_pk_mul_f32 v[160:161], v[128:129], v[160:161]\n\t"
      "v_pk_mul_f32 v[162:163], v[130:131], v[162:163]\n\t"
      "v_pk_mul_f32 v[164:165], v[132:133], v[164:165]\n\t"
      "v_pk_mul_f32 v[166:167], v[134:135], v[166:167]\n\t"
      "v_pk_mul_f32 v[168:169], v[136:137], v[168:169]\n\t"
      "v_pk_mul_f32 v[170:171], v[138:139], v[170:171]\n\t"
      "v_pk_mul_f32 v[172:173], v[140:141], v[172:173]\n\t"
      "v_pk_mul_f32 v[174:175], v[142:143], v[174:175]\n\t"
      "v_cvt_pk_bf16_f32 v176, v160, v161\n\t"
      "v_cvt_pk_bf16_f32 v177, v162, v163\n\t"
      "v_cvt_pk_bf16_f32 v178, v164, v165\n\t"
      "v_cvt_pk_bf16_f32 v179, v166, v167\n\t"
      "v_cvt_pk_bf16_f32 v180, v168, v169\n\t"
      "v_cvt_pk_bf16_f32 v181, v170, v171\n\t"
      "v_cvt_pk_bf16_f32 v182, v172, v173\n\t"
      "v_cvt_pk_bf16_f32 v183, v174, v175\n\t"
      "s_waitcnt lgkmcnt(4)\n\t"
      "s_cmp_lt_i32 %[k], 1\n\t"
      "s_cbranch_scc1 Lnd8_%=\n\t"
      "s_add_u32 m0, %[ring], 0\n\t"
      "s_nop 0\n\t"
      "buffer_load_dwordx4 v[216:217], %[srd], 0 idxen offen lds\n\t"
      "s_add_u32 m0, %[ring], 1024\n\t"
      "s_nop 0\n\t"
      "buffer_load_dwordx4 v[218:219], %[srd], 0 idxen offen lds\n\t"
      "s_add_u32 m0, %[ring], 2048\n\t"
      "s_nop 0\n\t"
      "buffer_load_dwordx4 v[220:221], %[srd], 0 idxen offen lds\n\t"
      "s_add_u32 m0, %[ring], 3072\n\t"
      "s_nop 0\n\t"
      "buffer_load_dwordx4 v[222:223], %[srd], 0 idxen offen lds\n\t"
      "s_branch Lge8_%=\n\t"
      "Lnd8_%=:\n\t"
      "s_cmp_lg_i32 %[k], 0\n\t"
      "s_cbranch_scc1 Lge8_%=\n\t"
      "global_load_dwordx4 v[224:227], %[wuo], %[wub] offset:-4096\n\t"
      "global_load_dwordx4 v[228:231], %[wuo], %[wub] offset:-3072\n\t"
      "global_load_dwordx4 v[232:235], %[wuo], %[wub] offset:-2048\n\t"
      "global_load_dwordx4 v[236:239], %[wuo], %[wub] offset:-1024\n\t"
      "global_load_dwordx4 v[240:243], %[wuo], %[wub] offset:0\n\t"
      "global_load_dwordx4 v[244:247], %[wuo], %[wub] offset:1024\n\t"
      "global_load_dwordx4 v[248:251], %[wuo], %[wub] offset:2048\n\t"
      "global_load_dwordx4 v[252:255], %[wuo], %[wub] offset:3072\n\t"
      "Lge8_%=:\n\t"
      "s_sub_i32 %[k], %[k], 1\n\t"
      "v_exp_f32 v160, v144\n\t"
      "v_mfma_f32_32x32x16_bf16 v[128:143], v[200:203], %[q00], 0\n\t"
      "v_exp_f32 v161, v145\n\t"
      "v_exp_f32 v162, v146\n\t"
      "v_exp_f32 v163, v147\n\t"
      "v_exp_f32 v164, v148\n\t"
      "v_mfma_f32_32x32x16_bf16 v[128:143], v[204:207], %[q01], v[128:143]\n\t"
      "v_exp_f32 v165, v149\n\t"
      "v_exp_f32 v166, v150\n\t"
      "v_exp_f32 v167, v151\n\t"
      "v_exp_f32 v168, v152\n\t"
      "v_mfma_f32_32x32x16_bf16 v[128:143], v[184:187], %[id0], v[128:143]\n\t"
      "v_exp_f32 v169, v153\n\t"
      "v_exp_f32 v170, v154\n\t"
      "v_exp_f32 v171, v155\n\t"
      "v_exp_f32 v172, v156\n\t"
      "v_exp_f32 v173, v157\n\t"
      "v_exp_f32 v174, v158\n\t"
      "v_exp_f32 v175, v159\n\t"
      "v_pk_add_f32 v[160:161], v[160:161], 1.0 op_sel_hi:[1,0]\n\t"
      "v_pk_add_f32 v[162:163], v[162:163], 1.0 op_sel_hi:[1,0]\n\t"
      "v_pk_add_f32 v[164:165], v[164:165], 1.0 op_sel_hi:[1,0]\n\t"
      "v_pk_add_f32 v[166:167], v[166:167], 1.0 op_sel_hi:[1,0]\n\t"
      "v_pk_add_f32 v[168:169], v[168:169], 1.0 op_sel_hi:[1,0]\n\t"
      "v_pk_add_f32 v[170:171], v[170:171], 1.0 op_sel_hi:[1,0]\n\t"
      "v_pk_add_f32 v[172:173], v[172:173], 1.0 op_sel_hi:[1,0]\n\t"
      "v_pk_add_f32 v[174:175], v[174:175], 1.0 op_sel_hi:[1,0]\n\t"
      "v_rcp_f32 v160, v160\n\t"
      "v_mfma_f32_32x32x16_bf16 v[128:143], v[188:191], %[id1], v[128:143]\n\t"
      "v_rcp_f32 v161, v161\n\t"
      "s_waitcnt lgkmcnt(0)\n\t"
      "v_rcp_f32 v162, v162\n\t"
      "v_rcp_f32 v163, v163\n\t"
      "v_rcp_f32 v164, v164\n\t"
      "v_mfma_f32_32x32x16_bf16 %[ag0], v[176:179], v[208:211], %[ag0]\n\t"
      "v_rcp_f32 v165, v165\n\t"
      "v_rcp_f32 v166, v166\n\t"
      "v_rcp_f32 v167, v167\n\t"
      "v_rcp_f32 v168, v168\n\t"
      "v_mfma_f32_32x32x16_bf16 %[ag0], v[180:183], v[212:215], %[ag0]\n\t"
      "v_rcp_f32 v169, v169\n\t"
      "v_rcp_f32 v170, v170\n\t"
      "v_rcp_f32 v171, v171\n\t"
      "v_rcp_f32 v172, v172\n\t"
      "v_rcp_f32 v173, v173\n\t"
      "v_rcp_f32 v174, v174\n\t"
      "v_rcp_f32 v175, v175\n\t"
      "v_pk_mul_f32 v[160:161], v[144:145], v[160:161]\n\t"
      "v_pk_mul_f32 v[162:163], v[146:147], v[162:163]\n\t"
      "v_pk_mul_f32 v[164:165], v[148:149], v[164:165]\n\t"
      "v_pk_mul_f32 v[166:167], v[150:151], v[166:167]\n\t"
      "v_pk_mul_f32 v[168:169], v[152:153], v[168:169]\n\t"
      "v_pk_mul_f32 v[170:171], v[154:155], v[170:171]\n\t"
      "v_pk_mul_f32 v[172:173], v[156:157], v[172:173]\n\t"
      "v_pk_mul_f32 v[174:175], v[158:159], v[174:175]\n\t"
      "v_cvt_pk_bf16_f32 v176, v160, v161\n\t"
      "v_cvt_pk_bf16_f32 v177, v162, v163\n\t"
      "v_cvt_pk_bf16_f32 v178, v164, v165\n\t"
      "v_cvt_pk_bf16_f32 v179, v166, v167\n\t"
      "v_cvt_pk_bf16_f32 v180, v168, v169\n\t"
      "v_cvt_pk_bf16_f32 v181, v170, v171\n\t"
      "v_cvt_pk_bf16_f32 v182, v172, v173\n\t"
      "v_cvt_pk_bf16_f32 v183, v174, v175\n\t"
      "v_add_u32 %[sa0], 0x1000, %[sa0]\n\t"
      "v_add_u32 %[sa1], 0x1000, %[sa1]\n\t"
      "v_add_u32 %[st0], 0x1000, %[st0]\n\t"
      "v_add_u32 %[st1], 0x1000, %[st1]\n\t"
      "v_add_u32 %[lst], 0x100, %[lst]\n\t"
      "s_sub_u32 %[t], %[t], 1\n\t"
      "s_cmp_lg_u32 %[t], 0\n\t"
      "s_cbranch_scc1 Lloop_%=\n\t"
      "s_branch Ltail_%=\n\t"
      "Lexit_even_%=:\n\t"
      "v_add_u32 %[st0], 0x800, %[st0]\n\t"
      "v_add_u32 %[st1], 0x800, %[st1]\n\t"
      "Ltail_%=:\n\t"
      "s_nop 1\n\t"
      "v_mfma_f32_32x32x16_bf16 %[ag1], v[176:179], v[208:211], %[ag1]\n\t"
      "v_mfma_f32_32x32x16_bf16 %[ag1], v[180:183], v[212:215], %[ag1]\n\t"
      "s_cmp_eq_u32 %[m], 0\n\t"
      "s_cbranch_scc1 Ldone_%=\n\t"
      "ds_read_b64_tr_b16 v[208:209], %[st0] offset:0\n\t"
      "ds_read_b64_tr_b16 v[210:211], %[st1] offset:0\n\t"
      "ds_read_b64_tr_b16 v[212:213], %[st0] offset:1024\n\t"
      "ds_read_b64_tr_b16 v[214:215], %[st1] offset:1024\n\t"
      "v_mfma_f32_32x32x16_bf16 v[144:159], v[200:203], %[q10], 0\n\t"
      "v_mfma_f32_32x32x16_bf16 v[144:159], v[204:207], %[q11], v[144:159]\n\t"
      "v_mov_b32 v178, 0\n\t"
      "v_mov_b32 v179, 0\n\t"
      "v_mov_b32 v180, 0\n\t"
      "v_mov_b32 v181, 0\n\t"
      "v_mov_b32 v182, 0\n\t"
      "v_mov_b32 v183, 0\n\t"
      "v_mfma_f32_32x32x16_bf16 v[144:159], v[192:195], %[id0], v[144:159]\n\t"
      "v_mfma_f32_32x32x16_bf16 v[144:159], v[196:199], %[id1], v[144:159]\n\t"
      "v_exp_f32 v160, v128\n\t"
      "v_exp_f32 v161, v129\n\t"
      "v_exp_f32 v162, v130\n\t"
      "v_exp_f32 v163, v131\n\t"
      "s_nop 0\n\t"
      "v_add_f32 v160, 1.0, v160\n\t"
      "v_add_f32 v161, 1.0, v161\n\t"
      "v_add_f32 v162, 1.0, v162\n\t"
      "v_add_f32 v163, 1.0, v163\n\t"
      "v_rcp_f32 v160, v160\n\t"
      "v_rcp_f32 v161, v161\n\t"
      "v_rcp_f32 v162, v162\n\t"
      "v_rcp_f32 v163, v163\n\t"
      "s_nop 0\n\t"
      "v_mul_f32 v160, v128, v160\n\t"
      "v_mul_f32 v161, v129, v161\n\t"
      "v_mul_f32 v162, v130, v162\n\t"
      "v_mul_f32 v163, v131, v163\n\t"
      "v_cvt_pk_bf16_f32 v176, v160, v161\n\t"
      "v_cvt_pk_bf16_f32 v177, v162, v163\n\t"
      "s_cmp_lt_u32 %[m], 2\n\t"
      "s_cbranch_scc1 Lqa_%=\n\t"
      "v_exp_f32 v164, v132\n\t"
      "v_exp_f32 v165, v133\n\t"
      "v_exp_f32 v166, v134\n\t"
      "v_exp_f32 v167, v135\n\t"
      "s_nop 0\n\t"
      "v_add_f32 v164, 1.0, v164\n\t"
      "v_add_f32 v165, 1.0, v165\n\t"
      "v_add_f32 v166, 1.0, v166\n\t"
      "v_add_f32 v167, 1.0, v167\n\t"
      "v_rcp_f32 v164, v164\n\t"
      "v_rcp_f32 v165, v165\n\t"
      "v_rcp_f32 v166, v166\n\t"
      "v_rcp_f32 v167, v167\n\t"
      "s_nop 0\n\t"
      "v_mul_f32 v164, v132, v164\n\t"
      "v_mul_f32 v165, v133, v165\n\t"
      "v_mul_f32 v166, v134, v166\n\t"
      "v_mul_f32 v167, v135, v167\n\t"
      "v_cvt_pk_bf16_f32 v178, v164, v165\n\t"
      "v_cvt_pk_bf16_f32 v179, v166, v167\n\t"
      "s_cmp_lt_u32 %[m], 3\n\t"
      "s_cbranch_scc1 Lqa_%=\n\t"
      "v_exp_f32 v168, v136\n\t"
      "v_exp_f32 v169, v137\n\t"
      "v_exp_f32 v170, v138\n\t"
      "v_exp_f32 v171, v139\n\t"
      "s_nop 0\n\t"
      "v_add_f32 v168, 1.0, v168\n\t"
      "v_add_f32 v169, 1.0, v169\n\t"
      "v_add_f32 v170, 1.0, v170\n\t"
      "v_add_f32 v171, 1.0, v171\n\t"
      "v_rcp_f32 v168, v168\n\t"
      "v_rcp_f32 v169, v169\n\t"
      "v_rcp_f32 v170, v170\n\t"
      "v_rcp_f32 v171, v171\n\t"
      "s_nop 0\n\t"
      "v_mul_f32 v168, v136, v168\n\t"
      "v_mul_f32 v169, v137, v169\n\t"
      "v_mul_f32 v170, v138, v170\n\t"
      "v_mul_f32 v171, v139, v171\n\t"
      "v_cvt_pk_bf16_f32 v180, v168, v169\n\t"
      "v_cvt_pk_bf16_f32 v181, v170, v171\n\t"
      "Lqa_%=:\n\t"
      "s_waitcnt lgkmcnt(0)\n\t"
      "s_nop 1\n\t"
      "v_mfma_f32_32x32x16_bf16 %[ag0], v[176:179], v[208:211], %[ag0]\n\t"
      "v_mfma_f32_32x32x16_bf16 %[ag0], v[180:183], v[212:215], %[ag0]\n\t"
      "v_exp_f32 v160, v144\n\t"
      "v_exp_f32 v161, v145\n\t"
      "v_exp_f32 v162, v146\n\t"
      "v_exp_f32 v163, v147\n\t"
      "s_nop 0\n\t"
      "v_add_f32 v160, 1.0, v160\n\t"
      "v_add_f32 v161, 1.0, v161\n\t"
      "v_add_f32 v162, 1.0, v162\n\t"
      "v_add_f32 v163, 1.0, v163\n\t"
      "v_rcp_f32 v160, v160\n\t"
      "v_rcp_f32 v161, v161\n\t"
      "v_rcp_f32 v162, v162\n\t"
      "v_rcp_f32 v163, v163\n\t"
      "s_nop 0\n\t"
      "v_mul_f32 v160, v144, v160\n\t"
      "v_mul_f32 v161, v145, v161\n\t"
      "v_mul_f32 v162, v146, v162\n\t"
      "v_mul_f32 v163, v147, v163\n\t"
      "v_cvt_pk_bf16_f32 v176, v160, v161\n\t"
      "v_cvt_pk_bf16_f32 v177, v162, v163\n\t"
      "s_cmp_lt_u32 %[m], 2\n\t"
      "s_cbranch_scc1 Lqb_%=\n\t"
      "v_exp_f32 v164, v148\n\t"
      "v_exp_f32 v165, v149\n\t"
      "v_exp_f32 v166, v150\n\t"
      "v_exp_f32 v167, v151\n\t"
      "s_nop 0\n\t"
      "v_add_f32 v164, 1.0, v164\n\t"
      "v_add_f32 v165, 1.0, v165\n\t"
      "v_add_f32 v166, 1.0, v166\n\t"
      "v_add_f32 v167, 1.0, v167\n\t"
      "v_rcp_f32 v164, v164\n\t"
      "v_rcp_f32 v165, v165\n\t"
      "v_rcp_f32 v166, v166\n\t"
      "v_rcp_f32 v167, v167\n\t"
      "s_nop 0\n\t"
      "v_mul_f32 v164, v148, v164\n\t"
      "v_mul_f32 v165, v149, v165\n\t"
      "v_mul_f32 v166, v150, v166\n\t"
      "v_mul_f32 v167, v151, v167\n\t"
      "v_cvt_pk_bf16_f32 v178, v164, v165\n\t"
      "v_cvt_pk_bf16_f32 v179, v166, v167\n\t"
      "s_cmp_lt_u32 %[m], 3\n\t"
      "s_cbranch_scc1 Lqb_%=\n\t"
      "v_exp_f32 v168, v152\n\t"
      "v_exp_f32 v169, v153\n\t"
      "v_exp_f32 v170, v154\n\t"
      "v_exp_f32 v171, v155\n\t"
      "s_nop 0\n\t"
      "v_add_f32 v168, 1.0, v168\n\t"
      "v_add_f32 v169, 1.0, v169\n\t"
      "v_add_f32 v170, 1.0, v170\n\t"
      "v_add_f32 v171, 1.0, v171\n\t"
      "v_rcp_f32 v168, v168\n\t"
      "v_rcp_f32 v169, v169\n\t"
      "v_rcp_f32 v170, v170\n\t"
      "v_rcp_f32 v171, v171\n\t"
      "s_nop 0\n\t"
      "v_mul_f32 v168, v152, v168\n\t"
      "v_mul_f32 v169, v153, v169\n\t"
      "v_mul_f32 v170, v154, v170\n\t"
      "v_mul_f32 v171, v155, v171\n\t"
      "v_cvt_pk_bf16_f32 v180, v168, v169\n\t"
      "v_cvt_pk_bf16_f32 v181, v170, v171\n\t"
      "Lqb_%=:\n\t"
      "s_nop 1\n\t"
      "v_mfma_f32_32x32x16_bf16 %[ag1], v[176:179], v[208:211], %[ag1]\n\t"
      "v_mfma_f32_32x32x16_bf16 %[ag1], v[180:183], v[212:215], %[ag1]\n\t"
      "Ldone_%=:\n\t"
      "s_mov_b32 m0, %[keep]\n\t"
      "s_waitcnt vmcnt(0) lgkmcnt(0)\n\t"
      "s_nop 11\n\t"
      : [ag0] "+v"(agg0), [ag1] "+v"(agg1), "=&{v[224:239]}"(wu0), "=&{v[240:255]}"(wu1), [sa0] "+v"(sa0), [sa1] "+v"(sa1),
        [st0] "+v"(st0), [st1] "+v"(st1), [lst] "+v"(lst), [k] "+s"(k), [t] "=&s"(t), [keep] "=&s"(keep)
      : [q00] "v"(q00), [q01] "v"(q01), [q10] "v"(q10), [q11] "v"(q11), [id0] "v"(id0), [id1] "v"(id1), [pa0] "v"(pa0),
        [pa1] "v"(pa0 ^ 32u), [pa2] "v"(pa0 ^ 64u), [pa3] "v"(pa0 ^ 96u), [s0] "v"(s0), [s1] "v"(s0 ^ 64u), [wuo] "v"(wuo),
        [srd] "s"(srd), [wub] "s"(wub), [ring] "s"(ring), [nf] "s"(nf), [m] "s"(m)
      : "memory", "scc", "v128", "v129", "v130", "v131", "v132", "v133", "v134", "v135", "v136", "v137", "v138", "v139", "v140", "v141", "v142", "v143", "v144", "v145", "v146", "v147", "v148", "v149", "v150", "v151", "v152", "v153", "v154", "v155", "v156", "v157", "v158", "v159", "v160", "v161", "v162", "v163", "v164", "v165", "v166", "v167", "v168", "v169", "v170", "v171", "v172", "v173", "v174", "v175", "v176", "v177", "v178", "v179", "v180", "v181", "v182", "v183", "v184", "v185", "v186", "v187", "v188", "v189", "v190", "v191", "v192", "v193", "v194", "v195", "v196", "v197", "v198", "v199", "v200", "v201", "v202", "v203", "v204", "v205", "v206", "v207", "v208", "v209", "v210", "v211", "v212", "v213", "v214", "v215", "v216", "v217", "v218", "v219", "v220", "v221", "v222", "v223");
}

DI void put_record(char* lds, int sub, unsigned row, Rec r) {
  const unsigned rr = sub * CAP + row, sw = (rr >> 2) & 3u, k = r.a & 255u;
  char* srow = lds + L_S + rr * 64;
  *(unsigned*)(lds + L_LIST + rr * 4) = r.a >> 8;
  *(unsigned short*)(srow + (((k >> 3) ^ sw) << 4) + ((k & 7u) << 1)) = 0x3F80;
  u32x4 e = {r.e01, r.e23, r.e01, r.e23};
  *(u32x4*)(srow + ((3u ^ sw) << 4)) = e;
}

DI void clear_images(char* lds, int tid) {
  const u32x4 z = {0, 0, 0, 0}, o = {OOB, OOB, OOB, OOB};
#pragma unroll
  for (int i = 0; i < (L_LIST / 16 + 511) / 512; ++i)
    if (tid + i * 512 < L_LIST / 16) *(u32x4*)(lds + (tid + i * 512) * 16) = z;
  if (tid < (L_ZEND - L_LIST) / 16) *(u32x4*)(lds + L_LIST + tid * 16) = o;
}

__global__ __launch_bounds__(512, 2) void mpnn_edge(const unsigned short* __restrict__ x, const char* __restrict__ ws,
                                                    unsigned short* __restrict__ out) {
  __shared__ __attribute__((aligned(16))) char lds[L_TOTAL];
  const int tid = threadIdx.x, lane = tid & 63, w = __builtin_amdgcn_readfirstlane(tid >> 6);
  const int g = blockIdx.x, ord = (g & 7) * 32 + (g >> 3);
  const int sub = w >> 2, fh = w & 1, half = (w >> 1) & 1, w4 = w & 3;
  const int c = lane & 31, h = lane >> 5, pc = w4 * 16 + (lane >> 2);
  STAMP_DECL
  STAMP(0);

  const u32x2 tb = *(const u32x2*)(ws + WS_TAB + pc * 2048 + ord * 8);
  const unsigned ring = lds_addr(lds) + L_RING + w * 8192;
  {
    const u32x4 xs = make_srd(x, NN * 256), ws2 = make_srd(ws + WS_W2, 32768);
    const unsigned r4 = lane >> 4, c16 = lane & 15;
    unsigned row = w * 4 + r4;
    dma16(xs, (row * 256 + g) * 256 + ((c16 ^ (row & 15)) << 4), lds_addr(lds) + L_X + w * 1024);
    if (w < 4) {
      row += 32;
      dma16(xs, (row * 256 + g) * 256 + ((c16 ^ (row & 15)) << 4), lds_addr(lds) + L_X + (w + 8) * 1024);
    }
#pragma unroll
    for (int i = 0; i < 4; ++i) dma16(ws2, (w * 4 + i) * 1024 + lane * 16, ring + 4096 + i * 1024);
  }
  const int fbq = 2 * fh + half;
  const u32x4 w3 = *(const u32x4*)(ws + WS_W3 + (fbq * 32 + c) * 16);
  clear_images(lds, tid);
  STAMP(1);

  const unsigned sA = tb[0] & 0xffffu, sB = tb[0] >> 16, eB = tb[1];
  const unsigned first = sub ? sB : sA, cnt = sub ? eB - sB : sB - sA, l4 = lane & 3;
  const Rec* rp = (const Rec*)(ws + WS_REC) + pc * CHUNK + first;
  Rec rec[NREC];
#pragma unroll
  for (int i = 0; i < NREC; ++i) {
    rec[i] = Rec{0, 0, 0};
    if (l4 + 4 * i < cnt) rec[i] = rp[l4 + 4 * i];
  }
  const unsigned incl = wave_scan(l4 == 0 ? cnt : 0u);
  unsigned* wsum = (unsigned*)(lds + L_WSUM);
  if (lane == 63) wsum[w] = incl;
  STAMP(2);
  barrier_all();
  STAMP(3);
  const u32x4 wa = *(const u32x4*)wsum, wb = *(const u32x4*)(wsum + 4);
  const unsigned nA = wa[0] + wa[1] + wa[2] + wa[3], nB = wb[0] + wb[1] + wb[2] + wb[3];
  const u32x4 wm = sub ? wb : wa;
  const unsigned pos = (w4 > 0 ? wm[0] : 0) + (w4 > 1 ? wm[1] : 0) + (w4 > 2 ? wm[2] : 0) + incl - cnt;
  const int ntot = __builtin_amdgcn_readfirstlane(sub ? nB : nA);
  const int nmax = __builtin_amdgcn_readfirstlane(max(nA, nB));
#pragma unroll
  for (int i = 0; i < NREC; ++i)
    if (l4 + 4 * i < cnt && pos + l4 + 4 * i < CAP) put_record(lds, sub, pos + l4 + 4 * i, rec[i]);
  for (unsigned i = l4 + 4 * NREC; i < cnt && pos + i < CAP; i += 4) put_record(lds, sub, pos + i, rp[i]);

  bf16x8 qf[2][2];
  {
    const int xrow = sub * SUBN + min(c, SUBN - 1);
    const char* wq = lds + L_RING + 4096 + fbq * 16384 + lane * 16;
    f32x16 q = {};
#pragma unroll
    for (int s = 0; s < 8; ++s)
      q = mfma32(lds_frag(lds + L_X + 256 * xrow + 16 * ((2 * s + h) ^ (xrow & 15))),
                 lds_frag(wq + (s >> 2) * 8192 + (s & 3) * 1024), q);
    u32x4 a, b;
#pragma unroll
    for (int i = 0; i < 4; ++i) {
      a[i] = pack2(q[2 * i] * NEG_LOG2E, q[2 * i + 1] * NEG_LOG2E);
      b[i] = pack2(q[8 + 2 * i] * NEG_LOG2E, q[9 + 2 * i] * NEG_LOG2E);
      b[i] = h ? w3[i] : b[i];
    }
    char* xq = lds + L_RING + (w ^ 2) * 8192 + lane * 16;
    *(u32x4*)xq = a;
    *(u32x4*)(xq + 1024) = b;
    STAMP(4);
    barrier_lds();
    STAMP(5);
    const char* rq = lds + L_RING + w * 8192 + lane * 16;
    const u32x4 oa = *(const u32x4*)rq, ob = *(const u32x4*)(rq + 1024);
    qf[0][0] = as_frag(half ? oa : a);
    qf[0][1] = as_frag(half ? ob : b);
    qf[1][0] = as_frag(half ? a : oa);
    qf[1][1] = as_frag(half ? b : ob);
    asm volatile("s_waitcnt lgkmcnt(0)" : "+v"(qf[0][0]), "+v"(qf[0][1]), "+v"(qf[1][0]), "+v"(qf[1][1])::"memory");
  }

  bf16x8 id[2];
#pragma unroll
  for (int s = 0; s < 2; ++s) {
    u32x4 v;
#pragma unroll
    for (int i = 0; i < 4; ++i) {
      const int f0 = ((2 * i) & 3) + 16 * h + 8 * (i >> 1) + 4 * s;
      v[i] = (c == f0 ? 0x3F80u : 0u) | (c == f0 + 1 ? 0x3F800000u : 0u);
    }
    id[s] = as_frag(v);
  }

  u32x4 srd = make_srd(ws + WS_P, P_BYTES / 256);
  srd[1] |= 256u << 16;
  const unsigned s0 = fh * 128 + (((lane & 7) ^ (lane >> 4)) << 4);
  const unsigned pa0 = ring + c * 128 + ((h ^ ((c >> 1) & 7)) << 4);
  const unsigned lb = lds_addr(lds);
  u32x16 wu0, wu1;

  f32x16 agg0 = {}, agg1 = {};
  for (int base = 0;;) {
    const int nw = min(max(ntot - base, 0), CAP), oct = (nw + 7) >> 3;
    const int o0 = sub ? oct >> 1 : (oct + 1) >> 1;
    const int myo = half ? oct - o0 : o0, r0 = sub * CAP + (half ? o0 * 8 : 0);
    const int sr = r0 + c;
    const unsigned sa0 = lb + L_S + sr * 64 + ((h ^ ((sr >> 2) & 3)) << 4);
    const int tq = (lane & 15) >> 2, tp = ((lane & 1) << 1) | ((lane >> 1) & 1), tch = 2 * ((lane >> 4) & 1) + (tp >> 1);
    const int tr0 = r0 + 4 * h + tq, tr1 = tr0 + 8;
    const unsigned st0 = lb + L_S + tr0 * 64 + 16 * (tch ^ ((tr0 >> 2) & 3)) + 8 * (tp & 1);
    const unsigned st1 = lb + L_S + tr1 * 64 + 16 * (tch ^ ((tr1 >> 2) & 3)) + 8 * (tp & 1);
    STAMP(6);
    edge_tiles(agg0, agg1, wu0, wu1, qf[0][0], qf[0][1], qf[1][0], qf[1][1], id[0], id[1], pa0, sa0, sa0 ^ 32u, st0, st1,
               lb + L_LIST + (r0 + (lane >> 3)) * 4, s0, w * 8192 + lane * 16, srd, ws + WS_WU + 4096, ring, myo >> 2, myo & 3);
    base += CAP;
    if (base >= nmax) break;
    barrier_all();
    clear_images(lds, tid);
    barrier_lds();
    const unsigned skip = pos < (unsigned)base ? (base - pos) & ~3u : 0u;
    for (unsigned i = skip + l4; i < cnt && pos + i < (unsigned)(base + CAP); i += 4)
      if (pos + i >= (unsigned)base) put_record(lds, sub, pos + i - base, rp[i]);
    barrier_all();
  }
  STAMP(8);

  const f32x16 give = half ? agg0 : agg1, mine = half ? agg1 : agg0;
#pragma unroll
  for (int q = 0; q < 4; ++q)
    *(f32x4*)(lds + L_RING + w * 8192 + q * 1024 + lane * 16) =
        (f32x4){give[4 * q], give[4 * q + 1], give[4 * q + 2], give[4 * q + 3]};
  const int n16 = lane & 15, kq = lane >> 4;
  f32x4 z[3] = {};
#pragma unroll
  for (int ct = 0; ct < 3; ++ct) {
#pragma unroll
    for (int s = 0; s < 4; ++s)
      z[ct] = mfma16(sub_frag(wu0, s), lds_frag(lds + L_X + 256 * (16 * ct + n16) + 16 * ((4 * s + kq) ^ n16)), z[ct]);
    z[ct] *= NEG_LOG2E;
  }
  barrier_lds();
  {
    const int row = sub * SUBN + c;
#pragma unroll
    for (int q = 0; q < 4; ++q) {
      const f32x4 o = *(const f32x4*)(lds + L_RING + (w ^ 2) * 8192 + q * 1024 + lane * 16);
      const u32x2 v = {pack2(mine[4 * q] + o[0], mine[4 * q + 1] + o[1]), pack2(mine[4 * q + 2] + o[2], mine[4 * q + 3] + o[3])};
      if (c < SUBN) *(u32x2*)(lds + L_AGG + 256 * row + 16 * ((4 * fbq + q) ^ (row & 15)) + 8 * h) = v;
    }
  }
  barrier_lds();
  STAMP(9);
#pragma unroll
  for (int ct = 0; ct < 3; ++ct) {
    const int row = min(16 * ct + n16, 2 * SUBN - 1);
#pragma unroll
    for (int s = 0; s < 4; ++s)
      z[ct] = mfma16(sub_frag(wu1, s), lds_frag(lds + L_AGG + 256 * row + 16 * ((4 * s + kq) ^ (row & 15))), z[ct]);
    float v[4];
#pragma unroll
    for (int i = 0; i < 4; ++i)
      v[i] = z[ct][i] * __builtin_amdgcn_rcpf(__builtin_fmaf(__builtin_amdgcn_exp2f(z[ct][i]), NEG_LOG2E, NEG_LOG2E));
    *(u32x2*)(lds + L_OUT + 256 * (16 * ct + n16) + 16 * ((2 * w + (kq >> 1)) ^ n16) + 8 * (kq & 1)) =
        (u32x2){pack2(v[0], v[1]), pack2(v[2], v[3])};
  }
  barrier_lds();
#pragma unroll
  for (int i = 0; i < 2; ++i) {
    const int k = tid + 512 * i, row = k >> 4, node = row * 256 + g;
    if (row < 2 * SUBN && node < NN)
      *(u32x4*)(out + (size_t)node * 128 + (k & 15) * 8) = *(const u32x4*)(lds + L_OUT + 256 * row + 16 * ((k & 15) ^ (row & 15)));
  }
  STAMP(10);
  STAMPW(11);
  STAMP_OUT(1, STW);
}

}

extern "C" void kernel_launch(void* const* d_in, const int* in_sizes, int n_in, void* d_out, int out_size, void* d_ws,
                              size_t ws_size, hipStream_t stream) {
  const unsigned short* x = (const unsigned short*)d_in[0];
  mpnn_prep<<<K1_GRID, 512, 0, stream>>>(x, (const u32x2*)d_in[1], (const int*)d_in[2], (const int*)d_in[3],
                                         (const unsigned short*)d_in[4], (const unsigned short*)d_in[5], (char*)d_ws);
  mpnn_edge<<<256, 512, 0, stream>>>(x, (const char*)d_ws, (unsigned short*)d_out);
}
```

```cpp
#include <hip/hip_runtime.h>
#include <hip/hip_bf16.h>
#include <stdint.h>

namespace {

typedef __attribute__((ext_vector_type(8))) __bf16 bf16x8;
typedef __attribute__((ext_vector_type(2))) __bf16 bf16x2;
typedef __attribute__((ext_vector_type(4))) short s16x4;
typedef __attribute__((ext_vector_type(16))) float f32x16;
typedef __attribute__((ext_vector_type(4))) float f32x4;
typedef __attribute__((ext_vector_type(2))) float f32x2;
typedef __attribute__((ext_vector_type(16))) unsigned u32x16;
typedef __attribute__((ext_vector_type(4))) unsigned u32x4;
typedef __attribute__((ext_vector_type(2))) unsigned u32x2;
struct __attribute__((packed, aligned(4))) Rec {
  unsigned a, e01, e23;
};
#define LDS_AS __attribute__((address_space(3)))
#define DI __device__ __forceinline__

constexpr int NN = 10000, NE = 160000;
constexpr int NPROD = 64, CHUNK = NE / NPROD;
constexpr int PBLK = (NN + 63) / 64;
constexpr int SUBN = 20;
constexpr float NEG_LOG2E = -1.4426950408889634f;

constexpr size_t WS_P = 0;
constexpr unsigned P_BYTES = PBLK * 64 * 256;
constexpr size_t WS_REC = WS_P + P_BYTES;
constexpr size_t WS_TAB = WS_REC + (size_t)NE * 12;
constexpr size_t WS_W2 = WS_TAB + NPROD * 2048;
constexpr size_t WS_WU = WS_W2 + 32768;
constexpr size_t WS_W3 = WS_WU + 65536;

#define STAMP_DECL
#define STAMP(i)
#define STAMPW(i)
#define STAMP_OUT(k, wv)

constexpr int K1_P0 = NPROD, K1_WT0 = K1_P0 + PBLK, K1_GRID = K1_WT0 + 13;

DI f32x16 mfma32(bf16x8 a, bf16x8 b, f32x16 c) { return __builtin_amdgcn_mfma_f32_32x32x16_bf16(a, b, c, 0, 0, 0); }
DI f32x4 mfma16(bf16x8 a, bf16x8 b, f32x4 c) { return __builtin_amdgcn_mfma_f32_16x16x32_bf16(a, b, c, 0, 0, 0); }
DI unsigned pack2(float a, float b) {
  bf16x2 v = {(__bf16)a, (__bf16)b};
  return __builtin_bit_cast(unsigned, v);
}
DI bf16x8 as_frag(u32x4 v) { return __builtin_bit_cast(bf16x8, v); }
DI bf16x8 sub_frag(const u32x16& v, int s) { return as_frag((u32x4){v[4 * s], v[4 * s + 1], v[4 * s + 2], v[4 * s + 3]}); }
DI bf16x8 lds_frag(const char* p) { return as_frag(*(const u32x4*)p); }
DI float bf2f(unsigned short u) { return __uint_as_float((unsigned)u << 16); }
DI unsigned swap23(unsigned v) { return (v & ~12u) | ((v & 4u) << 1) | ((v & 8u) >> 1); }
DI unsigned lds_addr(const char* p) { return (unsigned)(size_t)(const LDS_AS char*)p; }
DI unsigned wave_scan(unsigned v) {
  v += __builtin_amdgcn_update_dpp(0u, v, 0x111, 0xf, 0xf, false);
  v += __builtin_amdgcn_update_dpp(0u, v, 0x112, 0xf, 0xf, false);
  v += __builtin_amdgcn_update_dpp(0u, v, 0x114, 0xf, 0xf, false);
  v += __builtin_amdgcn_update_dpp(0u, v, 0x118, 0xf, 0xf, false);
  v += __builtin_amdgcn_update_dpp(0u, v, 0x142, 0xa, 0xf, false);
  v += __builtin_amdgcn_update_dpp(0u, v, 0x143, 0xc, 0xf, false);
  return v;
}
DI bf16x8 tr_frag(const char* lo, const char* hi) {
  s16x4 a = __builtin_amdgcn_ds_read_tr16_b64_v4i16((LDS_AS s16x4*)lo);
  s16x4 b = __builtin_amdgcn_ds_read_tr16_b64_v4i16((LDS_AS s16x4*)hi);
  return __builtin_bit_cast(bf16x8, __builtin_shufflevector(a, b, 0, 1, 2, 3, 4, 5, 6, 7));
}
DI u32x4 make_srd(const void* base, unsigned bytes) {
  const uint64_t pb = (uint64_t)base;
  u32x4 srd;
  srd[0] = __builtin_amdgcn_readfirstlane((unsigned)pb);
  srd[1] = __builtin_amdgcn_readfirstlane((unsigned)(pb >> 32));
  srd[2] = bytes;
  srd[3] = 0x00020000u;
  return srd;
}
DI void dma16(u32x4 srd, unsigned voff, unsigned dst) {
  unsigned keep;
  asm volatile("s_mov_b32 %0, m0\n\ts_mov_b32 m0, %3\n\ts_nop 2\n\tbuffer_load_dwordx4 %1, %2, 0 offen lds\n\ts_mov_b32 m0, %0"
               : "=&s"(keep)
               : "v"(voff), "s"(srd), "s"(dst)
               : "memory");
}
DI void st16(void* p, u32x4 v) { asm volatile("global_store_dwordx4 %0, %1, off sc0 sc1\n\ts_nop 1" :: "v"(p), "v"(v) : "memory"); }
DI void barrier_lds() {
  asm volatile("s_waitcnt lgkmcnt(0)\n\ts_barrier" ::: "memory");
}
DI void barrier_all() {
  asm volatile("s_waitcnt vmcnt(0) lgkmcnt(0)\n\ts_barrier" ::: "memory");
}

constexpr int K1_LDS = 83968;

__global__ __launch_bounds__(512) void mpnn_prep(const unsigned short* __restrict__ x, const u32x2* __restrict__ eattr,
                                                 const int* __restrict__ src, const int* __restrict__ dst,
                                                 const unsigned short* __restrict__ wmsg,
                                                 const unsigned short* __restrict__ wupd, char* __restrict__ ws) {
  __shared__ __attribute__((aligned(16))) char lds[K1_LDS];
  const int tid = threadIdx.x, lane = tid & 63, w = __builtin_amdgcn_readfirstlane(tid >> 6);
  const int b = blockIdx.x;
  STAMP_DECL
  STAMP(0);
  if (b < K1_P0) {
    const int e0 = b * CHUNK;
    unsigned* hist = (unsigned*)lds;
    unsigned short* start = (unsigned short*)(lds + 2048 + w * 1024);
    unsigned* stage = (unsigned*)(lds + 10240);
    int dv[5], sv[5];
    u32x2 ev[5];
#pragma unroll
    for (int i = 0; i < 5; ++i) dv[i] = dst[e0 + min(tid + 512 * i, CHUNK - 1)];
#pragma unroll
    for (int i = 0; i < 5; ++i) {
      const int k = min(tid + 512 * i, CHUNK - 1);
      sv[i] = src[e0 + k];
      ev[i] = eattr[e0 + k];
    }
    hist[tid] = 0;
    barrier_lds();
    unsigned key[5], rank[5];
#pragma unroll
    for (int i = 0; i < 5; ++i) {
      const unsigned d = dv[i], g = d & 255u, j = d >> 8, sub = j >= SUBN;
      key[i] = (((g & 7u) << 5) + (g >> 3)) * 2 + sub;
      dv[i] = swap23(j - SUBN * sub);
      rank[i] = 0;
      if (tid + 512 * i < CHUNK) rank[i] = atomicAdd(&hist[key[i]], 1u);
    }
    STAMP(1);
    barrier_lds();
    const u32x4 c0 = *(const u32x4*)(hist + 8 * lane), c1 = *(const u32x4*)(hist + 8 * lane + 4);
    const unsigned tot = c0[0] + c0[1] + c0[2] + c0[3] + c1[0] + c1[1] + c1[2] + c1[3];
    unsigned s[9];
    s[0] = wave_scan(tot) - tot;
#pragma unroll
    for (int i = 0; i < 4; ++i) s[i + 1] = s[i] + c0[i];
#pragma unroll
    for (int i = 0; i < 4; ++i) s[i + 5] = s[i + 4] + c1[i];
    const u32x4 sp = {s[0] | (s[1] << 16), s[2] | (s[3] << 16), s[4] | (s[5] << 16), s[6] | (s[7] << 16)};
    *(u32x4*)(start + 8 * lane) = sp;
    if (w < 2) {
      const u32x4 t = w ? (u32x4){sp[2], s[6], sp[3], s[8]} : (u32x4){sp[0], s[2], sp[1], s[4]};
      *(u32x4*)(ws + WS_TAB + b * 2048 + lane * 32 + w * 16) = t;
    }
    __builtin_amdgcn_wave_barrier();
#pragma unroll
    for (int i = 0; i < 5; ++i) {
      if (tid + 512 * i < CHUNK) {
        unsigned* r = stage + 3 * (start[key[i]] + rank[i]);
        r[0] = ((unsigned)sv[i] << 8) | dv[i];
        r[1] = ev[i][0];
        r[2] = ev[i][1];
      }
    }
    STAMP(2);
    barrier_lds();
    u32x4* go = (u32x4*)(ws + WS_REC + (size_t)e0 * 12);
#pragma unroll
    for (int i = 0; i < 4; ++i) {
      const int k = tid + 512 * i;
      if (k < CHUNK * 12 / 16) go[k] = ((const u32x4*)stage)[k];
    }
    STAMP(3);
    STAMPW(4);
  } else if (b < K1_WT0) {
    const int n0 = (b - K1_P0) * 64;
    char* wimg = lds;
    char* ximg = lds + 32768;
    char* oimg = lds + 49152;
    const u32x4 xs = make_srd(x, NN * 256), wsd = make_srd(wmsg, 128 * 256);
    const unsigned r4 = lane >> 4, c16 = lane & 15;
#pragma unroll
    for (int i = 0; i < 2; ++i) {
      const unsigned row = (w * 2 + i) * 4 + r4;
      dma16(xs, (n0 + row) * 256 + ((c16 ^ (row & 15)) << 4), lds_addr(ximg) + (w * 2 + i) * 1024);
    }
#pragma unroll
    for (int i = 0; i < 4; ++i) {
      const unsigned row = (w * 4 + i) * 4 + r4;
      dma16(wsd, row * 256 + ((c16 ^ (((row & 3) << 2) | ((row >> 2) & 3))) << 4), lds_addr(wimg) + (w * 4 + i) * 1024);
    }
    barrier_all();
    STAMP(5);
    const int c = lane & 31, h = lane >> 5, fb = w & 3, xrow = (w >> 2) * 32 + c;
    const int blk = (lane >> 4) & 1, q = (lane & 15) >> 2, p = lane & 3, ch = 4 * fb + 2 * blk + (p >> 1);
    f32x16 acc = {};
#pragma unroll
    for (int s = 0; s < 8; ++s) {
      const int r0 = 16 * s + 8 * h + q, r1 = r0 + 4;
      const bf16x8 a = tr_frag(wimg + 256 * r0 + 16 * (ch ^ (((r0 & 3) << 2) | ((r0 >> 2) & 3))) + 8 * (p & 1),
                               wimg + 256 * r1 + 16 * (ch ^ (((r1 & 3) << 2) | ((r1 >> 2) & 3))) + 8 * (p & 1));
      acc = mfma32(a, lds_frag(ximg + 256 * xrow + 16 * ((2 * s + h) ^ (xrow & 15))), acc);
    }
    u32x4 o0, o1;
#pragma unroll
    for (int i = 0; i < 4; ++i) {
      o0[i] = pack2(acc[2 * i] * NEG_LOG2E, acc[2 * i + 1] * NEG_LOG2E);
      o1[i] = pack2(acc[8 + 2 * i] * NEG_LOG2E, acc[9 + 2 * i] * NEG_LOG2E);
    }
    *(u32x4*)(oimg + 256 * xrow + 16 * ((4 * fb + 2 * h) ^ (xrow & 15))) = o0;
    *(u32x4*)(oimg + 256 * xrow + 16 * ((4 * fb + 2 * h + 1) ^ (xrow & 15))) = o1;
    STAMP(6);
    barrier_lds();
#pragma unroll
    for (int i = 0; i < 2; ++i) {
      const int k = tid + 512 * i, row = k >> 4;
      *(u32x4*)(ws + WS_P + (size_t)n0 * 256 + k * 16) = *(const u32x4*)(oimg + 256 * row + 16 * ((k & 15) ^ (row & 15)));
    }
    STAMP(7);
    STAMPW(8);
  } else if (b < K1_GRID - 1) {
    const int t = (b - K1_WT0) * 512 + tid;
    const unsigned short* wp;
    if (t < 2048) wp = wmsg + (128 + 16 * ((t >> 6) & 7) + 8 * (lane >> 5)) * 128 + (t >> 9) * 32 + (lane & 31);
    else wp = wupd + (32 * ((t >> 6) & 7) + 8 * (lane >> 4)) * 128 + ((t - 2048) >> 9) * 16 + (lane & 15);
    unsigned v[8];
#pragma unroll
    for (int j = 0; j < 8; ++j) v[j] = wp[j * 128];
    u32x4 o = {v[0] | (v[1] << 16), v[2] | (v[3] << 16), v[4] | (v[5] << 16), v[6] | (v[7] << 16)};
    *(u32x4*)(ws + WS_W2 + (size_t)t * 16) = o;
    STAMPW(9);
  } else if (tid < 128) {
    float hi[4], lo[4];
#pragma unroll
    for (int k = 0; k < 4; ++k) {
      const float v = bf2f(wmsg[(256 + k) * 128 + tid]) * NEG_LOG2E;
      hi[k] = (float)(__bf16)v;
      lo[k] = v - hi[k];
    }
    u32x4 o = {pack2(lo[0], lo[1]), pack2(lo[2], lo[3]), pack2(hi[0], hi[1]), pack2(hi[2], hi[3])};
    *(u32x4*)(ws + WS_W3 + tid * 16) = o;
  }
  STAMP_OUT(0, 0);
}

constexpr int CAP = 416;
constexpr int L_S = 0;
constexpr int L_LIST = L_S + (2 * CAP + 32) * 64;
constexpr int L_ZEND = L_LIST + (2 * CAP + 32) * 4;
constexpr int L_WSUM = L_ZEND;
constexpr int L_X = L_WSUM + 128;
constexpr int L_RING = L_X + 48 * 256;
constexpr int L_TOTAL = L_RING + 8 * 8192;
constexpr int L_AGG = L_S, L_OUT = L_S + 48 * 256;
constexpr unsigned OOB = 0x40000000u;
constexpr int NREC = 5;

DI void edge_tiles(f32x16& agg0, f32x16& agg1, u32x16& wu0, u32x16& wu1, bf16x8 q00, bf16x8 q01, bf16x8 q10, bf16x8 q11,
                   bf16x8 id0, bf16x8 id1, unsigned pa0, unsigned sa0, unsigned sa1, unsigned st0, unsigned st1,
                   unsigned lst, unsigned s0, unsigned wuo, u32x4 srd, const char* wub, unsigned ring, int nf, int m) {
  int k = nf + (m > 0), t;
  unsigned keep;
  asm volatile(
      "s_waitcnt lgkmcnt(0)\n\t"
      "s_mov_b32 %[keep], m0\n\t"
      "ds_read_b32 v216, %[lst] offset:0\n\t"
      "ds_read_b32 v218, %[lst] offset:32\n\t"
      "ds_read_b32 v220, %[lst] offset:64\n\t"
      "ds_read_b32 v222, %[lst] offset:96\n\t"
      "ds_read_b32 v160, %[lst] offset:128\n\t"
      "ds_read_b32 v162, %[lst] offset:160\n\t"
      "ds_read_b32 v164, %[lst] offset:192\n\t"
      "ds_read_b32 v166, %[lst] offset:224\n\t"
      "ds_read_b32 v168, %[lst] offset:256\n\t"
      "ds_read_b32 v170, %[lst] offset:288\n\t"
      "ds_read_b32 v172, %[lst] offset:320\n\t"
      "ds_read_b32 v174, %[lst] offset:352\n\t"
      "v_mov_b32 v217, %[s0]\n\t"
      "v_mov_b32 v219, %[s1]\n\t"
      "v_mov_b32 v221, %[s0]\n\t"
      "v_mov_b32 v223, %[s1]\n\t"
      "v_mov_b32 v161, %[s0]\n\t"
      "v_mov_b32 v163, %[s1]\n\t"
      "v_mov_b32 v165, %[s0]\n\t"
      "v_mov_b32 v167, %[s1]\n\t"
      "v_mov_b32 v169, %[s0]\n\t"
      "v_mov_b32 v171, %[s1]\n\t"
      "v_mov_b32 v173, %[s0]\n\t"
      "v_mov_b32 v175, %[s1]\n\t"
      "s_waitcnt lgkmcnt(8)\n\t"
      "s_cmp_lt_i32 %[k], 1\n\t"
      "s_cbranch_scc1 Lnd1_%=\n\t"
      "s_add_u32 m0, %[ring], 0\n\t"
      "s_nop 0\n\t"
      "buffer_load_dwordx4 v[216:217], %[srd], 0 idxen offen lds\n\t"
      "s_add_u32 m0, %[ring], 1024\n\t"
      "s_nop 0\n\t"
      "buffer_load_dwordx4 v[218:219], %[srd], 0 idxen offen lds\n\t"
      "s_add_u32 m0, %[ring], 2048\n\t"
      "s_nop 0\n\t"
      "buffer_load_dwordx4 v[220:221], %[srd], 0 idxen offen lds\n\t"
      "s_add_u32 m0, %[ring], 3072\n\t"
      "s_nop 0\n\t"
      "buffer_load_dwordx4 v[222:223], %[srd], 0 idxen offen lds\n\t"
      "s_branch Lge1_%=\n\t"
      "Lnd1_%=:\n\t"
      "s_cmp_lg_i32 %[k], 0\n\t"
      "s_cbranch_scc1 Lge1_%=\n\t"
      "global_load_dwordx4 v[224:227], %[wuo], %[wub] offset:-4096\n\t"
      "global_load_dwordx4 v[228:231], %[wuo], %[wub] offset:-3072\n\t"
      "global_load_dwordx4 v[232:235], %[wuo], %[wub] offset:-2048\n\t"
      "global_load_dwordx4 v[236:239], %[wuo], %[wub] offset:-1024\n\t"
      "global_load_dwordx4 v[240:243], %[wuo], %[wub] offset:0\n\t"
      "global_load_dwordx4 v[244:247], %[wuo], %[wub] offset:1024\n\t"
      "global_load_dwordx4 v[248:251], %[wuo], %[wub] offset:2048\n\t"
      "global_load_dwordx4 v[252:255], %[wuo], %[wub] offset:3072\n\t"
      "Lge1_%=:\n\t"
      "s_sub_i32 %[k], %[k], 1\n\t"
      "s_waitcnt lgkmcnt(4)\n\t"
      "s_cmp_lt_i32 %[k], 1\n\t"
      "s_cbranch_scc1 Lnd2_%=\n\t"
      "s_add_u32 m0, %[ring], 4096\n\t"
      "s_nop 0\n\t"
      "buffer_load_dwordx4 v[160:161], %[srd], 0 idxen offen lds\n\t"
      "s_add_u32 m0, %[ring], 5120\n\t"
      "s_nop 0\n\t"
      "buffer_load_dwordx4 v[162:163], %[srd], 0 idxen offen lds\n\t"
      "s_add_u32 m0, %[ring], 6144\n\t"
      "s_nop 0\n\t"
      "buffer_load_dwordx4 v[164:165], %[srd], 0 idxen offen lds\n\t"
      "s_add_u32 m0, %[ring], 7168\n\t"
      "s_nop 0\n\t"
      "buffer_load_dwordx4 v[166:167], %[srd], 0 idxen offen lds\n\t"
      "s_branch Lge2_%=\n\t"
      "Lnd2_%=:\n\t"
      "s_cmp_lg_i32 %[k], 0\n\t"
      "s_cbranch_scc1 Lge2_%=\n\t"
      "global_load_dwordx4 v[224:227], %[wuo], %[wub] offset:-4096\n\t"
      "global_load_dwordx4 v[228:231], %[wuo], %[wub] offset:-3072\n\t"
      "global_load_dwordx4 v[232:235], %[wuo], %[wub] offset:-2048\n\t"
      "global_load_dwordx4 v[236:239], %[wuo], %[wub] offset:-1024\n\t"
      "global_load_dwordx4 v[240:243], %[wuo], %[wub] offset:0\n\t"
      "global_load_dwordx4 v[244:247], %[wuo], %[wub] offset:1024\n\t"
      "global_load_dwordx4 v[248:251], %[wuo], %[wub] offset:2048\n\t"
      "global_load_dwordx4 v[252:255], %[wuo], %[wub] offset:3072\n\t"
      "Lge2_%=:\n\t"
      "s_sub_i32 %[k], %[k], 1\n\t"
      "v_mov_b32 v176, 0\n\t"
      "v_mov_b32 v177, 0\n\t"
      "v_mov_b32 v178, 0\n\t"
      "v_mov_b32 v179, 0\n\t"
      "v_mov_b32 v180, 0\n\t"
      "v_mov_b32 v181, 0\n\t"
      "v_mov_b32 v182, 0\n\t"
      "v_mov_b32 v183, 0\n\t"
      "v_mov_b32 v208, 0\n\t"
      "v_mov_b32 v209, 0\n\t"
      "v_mov_b32 v210, 0\n\t"
      "v_mov_b32 v211, 0\n\t"
      "v_mov_b32 v212, 0\n\t"
      "v_mov_b32 v213, 0\n\t"
      "v_mov_b32 v214, 0\n\t"
      "v_mov_b32 v215, 0\n\t"
      "s_add_u32 %[t], %[nf], %[m]\n\t"
      "s_cmp_eq_u32 %[t], 0\n\t"
      "s_cbranch_scc1 Ldone_%=\n\t"
      "ds_read_b128 v[200:203], %[sa0] offset:0\n\t"
      "ds_read_b128 v[204:207], %[sa1] offset:0\n\t"
      "s_cmp_lt_i32 %[k], 0\n\t"
      "s_cbranch_scc1 Lw83_%=\n\t"
      "s_waitcnt vmcnt(4)\n\t"
      "Lw83_%=:\n\t"
      "s_waitcnt vmcnt(8)\n\t"
      "ds_read_b128 v[184:187], %[pa0] offset:0\n\t"
      "ds_read_b128 v[188:191], %[pa1] offset:0\n\t"
      "ds_read_b128 v[192:195], %[pa2] offset:0\n\t"
      "ds_read_b128 v[196:199], %[pa3] offset:0\n\t"
      "s_waitcnt lgkmcnt(4)\n\t"
      "v_mfma_f32_32x32x16_bf16 v[128:143], v[200:203], %[q00], 0\n\t"
      "v_mfma_f32_32x32x16_bf16 v[128:143], v[204:207], %[q01], v[128:143]\n\t"
      "s_waitcnt lgkmcnt(0)\n\t"
      "s_cmp_lt_i32 %[k], 1\n\t"
      "s_cbranch_scc1 Lnd4_%=\n\t"
      "s_add_u32 m0, %[ring], 0\n\t"
      "s_nop 0\n\t"
      "buffer_load_dwordx4 v[168:169], %[srd], 0 idxen offen lds\n\t"
      "s_add_u32 m0, %[ring], 1024\n\t"
      "s_nop 0\n\t"
      "buffer_load_dwordx4 v[170:171], %[srd], 0 idxen offen lds\n\t"
      "s_add_u32 m0, %[ring], 2048\n\t"
      "s_nop 0\n\t"
      "buffer_load_dwordx4 v[172:173], %[srd], 0 idxen offen lds\n\t"
      "s_add_u32 m0, %[ring], 3072\n\t"
      "s_nop 0\n\t"
      "buffer_load_dwordx4 v[174:175], %[srd], 0 idxen offen lds\n\t"
      "s_branch Lge4_%=\n\t"
      "Lnd4_%=:\n\t"
      "s_cmp_lg_i32 %[k], 0\n\t"
      "s_cbranch_scc1 Lge4_%=\n\t"
      "global_load_dwordx4 v[224:227], %[wuo], %[wub] offset:-4096\n\t"
      "global_load_dwordx4 v[228:231], %[wuo], %[wub] offset:-3072\n\t"
      "global_load_dwordx4 v[232:235], %[wuo], %[wub] offset:-2048\n\t"
      "global_load_dwordx4 v[236:239], %[wuo], %[wub] offset:-1024\n\t"
      "global_load_dwordx4 v[240:243], %[wuo], %[wub] offset:0\n\t"
      "global_load_dwordx4 v[244:247], %[wuo], %[wub] offset:1024\n\t"
      "global_load_dwordx4 v[248:251], %[wuo], %[wub] offset:2048\n\t"
      "global_load_dwordx4 v[252:255], %[wuo], %[wub] offset:3072\n\t"
      "Lge4_%=:\n\t"
      "s_sub_i32 %[k], %[k], 1\n\t"
      "v_mfma_f32_32x32x16_bf16 v[128:143], v[184:187], %[id0], v[128:143]\n\t"
      "v_mfma_f32_32x32x16_bf16 v[128:143], v[188:191], %[id1], v[128:143]\n\t"
      "s_mov_b32 %[t], %[nf]\n\t"
      "s_cmp_eq_u32 %[t], 0\n\t"
      "s_nop 9\n\t"
      "s_cbranch_scc1 Ltail_%=\n\t"
      "Lloop_%=:\n\t"
      "v_exp_f32 v160, v128\n\t"
      "v_mfma_f32_32x32x16_bf16 v[144:159], v[200:203], %[q10], 0\n\t"
      "v_exp_f32 v161, v129\n\t"
      "v_exp_f32 v162, v130\n\t"
      "v_exp_f32 v163, v131\n\t"
      "v_exp_f32 v164, v132\n\t"
      "v_mfma_f32_32x32x16_bf16 v[144:159], v[204:207], %[q11], v[144:159]\n\t"
      "v_exp_f32 v165, v133\n\t"
      "s_cmp_lt_i32 %[k], 0\n\t"
      "s_cbranch_scc1 Lw85_%=\n\t"
      "s_waitcnt vmcnt(4)\n\t"
      "Lw85_%=:\n\t"
      "s_waitcnt vmcnt(8)\n\t"
      "ds_read_b128 v[200:203], %[sa0] offset:2048\n\t"
      "ds_read_b128 v[204:207], %[sa1] offset:2048\n\t"
      "ds_read_b128 v[184:187], %[pa0] offset:4096\n\t"
      "ds_read_b128 v[188:191], %[pa1] offset:4096\n\t"
      "v_exp_f32 v166, v134\n\t"
      "v_exp_f32 v167, v135\n\t"
      "v_exp_f32 v168, v136\n\t"
      "v_mfma_f32_32x32x16_bf16 v[144:159], v[192:195], %[id0], v[144:159]\n\t"
      "v_exp_f32 v169, v137\n\t"
      "v_exp_f32 v170, v138\n\t"
      "v_exp_f32 v171, v139\n\t"
      "v_exp_f32 v172, v140\n\t"
      "v_exp_f32 v173, v141\n\t"
      "v_exp_f32 v174, v142\n\t"
      "v_exp_f32 v175, v143\n\t"
      "v_pk_add_f32 v[160:161], v[160:161], 1.0 op_sel_hi:[1,0]\n\t"
      "v_pk_add_f32 v[162:163], v[162:163], 1.0 op_sel_hi:[1,0]\n\t"
      "v_pk_add_f32 v[164:165], v[164:165], 1.0 op_sel_hi:[1,0]\n\t"
      "v_pk_add_f32 v[166:167], v[166:167], 1.0 op_sel_hi:[1,0]\n\t"
      "v_pk_add_f32 v[168:169], v[168:169], 1.0 op_sel_hi:[1,0]\n\t"
      "v_pk_add_f32 v[170:171], v[170:171], 1.0 op_sel_hi:[1,0]\n\t"
      "v_pk_add_f32 v[172:173], v[172:173], 1.0 op_sel_hi:[1,0]\n\t"
      "v_pk_add_f32 v[174:175], v[174:175], 1.0 op_sel_hi:[1,0]\n\t"
      "v_rcp_f32 v160, v160\n\t"
      "v_mfma_f32_32x32x16_bf16 v[144:159], v[196:199], %[id1], v[144:159]\n\t"
      "v_rcp_f32 v161, v161\n\t"
      "ds_read_b128 v[192:195], %[pa2] offset:4096\n\t"
      "ds_read_b128 v[196:199], %[pa3] offset:4096\n\t"
      "ds_read_b32 v216, %[lst] offset:384\n\t"
      "ds_read_b32 v218, %[lst] offset:416\n\t"
      "ds_read_b32 v220, %[lst] offset:448\n\t"
      "ds_read_b32 v222, %[lst] offset:480\n\t"
      "v_rcp_f32 v162, v162\n\t"
      "v_rcp_f32 v163, v163\n\t"
      "v_rcp_f32 v164, v164\n\t"
      "v_mfma_f32_32x32x16_bf16 %[ag1], v[176:179], v[208:211], %[ag1]\n\t"
      "v_rcp_f32 v165, v165\n\t"
      "v_rcp_f32 v166, v166\n\t"
      "v_rcp_f32 v167, v167\n\t"
      "v_rcp_f32 v168, v168\n\t"
      "v_mfma_f32_32x32x16_bf16 %[ag1], v[180:183], v[212:215], %[ag1]\n\t"
      "v_rcp_f32 v169, v169\n\t"
      "ds_read_b64_tr_b16 v[208:209], %[st0] offset:0\n\t"
      "ds_read_b64_tr_b16 v[210:211], %[st1] offset:0\n\t"
      "ds_read_b64_tr_b16 v[212:213], %[st0] offset:1024\n\t"
      "ds_read_b64_tr_b16 v[214:215], %[st1] offset:1024\n\t"
      "v_rcp_f32 v170, v170\n\t"
      "v_rcp_f32 v171, v171\n\t"
      "v_rcp_f32 v172, v172\n\t"
      "v_rcp_f32 v173, v173\n\t"
      "v_rcp_f32 v174, v174\n\t"
      "v_rcp_f32 v175, v175\n\t"
      "v_pk_mul_f32 v[160:161], v[128:129], v[160:161]\n\t"
      "v_pk_mul_f32 v[162:163], v[130:131], v[162:163]\n\t"
      "v_pk_mul_f32 v[164:165], v[132:133], v[164:165]\n\t"
      "v_pk_mul_f32 v[166:167], v[134:135], v[166:167]\n\t"
      "v_pk_mul_f32 v[168:169], v[136:137], v[168:169]\n\t"
      "v_pk_mul_f32 v[170:171], v[138:139], v[170:171]\n\t"
      "v_pk_mul_f32 v[172:173], v[140:141], v[172:173]\n\t"
      "v_pk_mul_f32 v[174:175], v[142:143], v[174:175]\n\t"
      "v_cvt_pk_bf16_f32 v176, v160, v161\n\t"
      "v_cvt_pk_bf16_f32 v177, v162, v163\n\t"
      "v_cvt_pk_bf16_f32 v178, v164, v165\n\t"
      "v_cvt_pk_bf16_f32 v179, v166, v167\n\t"
      "v_cvt_pk_bf16_f32 v180, v168, v169\n\t"
      "v_cvt_pk_bf16_f32 v181, v170, v171\n\t"
      "v_cvt_pk_bf16_f32 v182, v172, v173\n\t"
      "v_cvt_pk_bf16_f32 v183, v174, v175\n\t"
      "s_waitcnt lgkmcnt(4)\n\t"
      "s_cmp_lt_i32 %[k], 1\n\t"
      "s_cbranch_scc1 Lnd6_%=\n\t"
      "s_add_u32 m0, %[ring], 4096\n\t"
      "s_nop 0\n\t"
      "buffer_load_dwordx4 v[216:217], %[srd], 0 idxen offen lds\n\t"
      "s_add_u32 m0, %[ring], 5120\n\t"
      "s_nop 0\n\t"
      "buffer_load_dwordx4 v[218:219], %[srd], 0 idxen offen lds\n\t"
      "s_add_u32 m0, %[ring], 6144\n\t"
      "s_nop 0\n\t"
      "buffer_load_dwordx4 v[220:221], %[srd], 0 idxen offen lds\n\t"
      "s_add_u32 m0, %[ring], 7168\n\t"
      "s_nop 0\n\t"
      "buffer_load_dwordx4 v[222:223], %[srd], 0 idxen offen lds\n\t"
      "s_branch Lge6_%=\n\t"
      "Lnd6_%=:\n\t"
      "s_cmp_lg_i32 %[k], 0\n\t"
      "s_cbranch_scc1 Lge6_%=\n\t"
      "global_load_dwordx4 v[224:227], %[wuo], %[wub] offset:-4096\n\t"
      "global_load_dwordx4 v[228:231], %[wuo], %[wub] offset:-3072\n\t"
      "global_load_dwordx4 v[232:235], %[wuo], %[wub] offset:-2048\n\t"
      "global_load_dwordx4 v[236:239], %[wuo], %[wub] offset:-1024\n\t"
      "global_load_dwordx4 v[240:243], %[wuo], %[wub] offset:0\n\t"
      "global_load_dwordx4 v[244:247], %[wuo], %[wub] offset:1024\n\t"
      "global_load_dwordx4 v[248:251], %[wuo], %[wub] offset:2048\n\t"
      "global_load_dwordx4 v[252:255], %[wuo], %[wub] offset:3072\n\t"
      "Lge6_%=:\n\t"
      "s_sub_i32 %[k], %[k], 1\n\t"
      "v_exp_f32 v160, v144\n\t"
      "v_mfma_f32_32x32x16_bf16 v[128:143], v[200:203], %[q00], 0\n\t"
      "v_exp_f32 v161, v145\n\t"
      "v_exp_f32 v162, v146\n\t"
      "v_exp_f32 v163, v147\n\t"
      "v_exp_f32 v164, v148\n\t"
      "v_mfma_f32_32x32x16_bf16 v[128:143], v[204:207], %[q01], v[128:143]\n\t"
      "v_exp_f32 v165, v149\n\t"
      "v_exp_f32 v166, v150\n\t"
      "v_exp_f32 v167, v151\n\t"
      "v_exp_f32 v168, v152\n\t"
      "v_mfma_f32_32x32x16_bf16 v[128:143], v[184:187], %[id0], v[128:143]\n\t"
      "v_exp_f32 v169, v153\n\t"
      "v_exp_f32 v170, v154\n\t"
      "v_exp_f32 v171, v155\n\t"
      "v_exp_f32 v172, v156\n\t"
      "v_exp_f32 v173, v157\n\t"
      "v_exp_f32 v174, v158\n\t"
      "v_exp_f32 v175, v159\n\t"
      "v_pk_add_f32 v[160:161], v[160:161], 1.0 op_sel_hi:[1,0]\n\t"
      "v_pk_add_f32 v[162:163], v[162:163], 1.0 op_sel_hi:[1,0]\n\t"
      "v_pk_add_f32 v[164:165], v[164:165], 1.0 op_sel_hi:[1,0]\n\t"
      "v_pk_add_f32 v[166:167], v[166:167], 1.0 op_sel_hi:[1,0]\n\t"
      "v_pk_add_f32 v[168:169], v[168:169], 1.0 op_sel_hi:[1,0]\n\t"
      "v_pk_add_f32 v[170:171], v[170:171], 1.0 op_sel_hi:[1,0]\n\t"
      "v_pk_add_f32 v[172:173], v[172:173], 1.0 op_sel_hi:[1,0]\n\t"
      "v_pk_add_f32 v[174:175], v[174:175], 1.0 op_sel_hi:[1,0]\n\t"
      "v_rcp_f32 v160, v160\n\t"
      "v_mfma_f32_32x32x16_bf16 v[128:143], v[188:191], %[id1], v[128:143]\n\t"
      "v_rcp_f32 v161, v161\n\t"
      "s_waitcnt lgkmcnt(0)\n\t"
      "v_rcp_f32 v162, v162\n\t"
      "v_rcp_f32 v163, v163\n\t"
      "v_rcp_f32 v164, v164\n\t"
      "v_mfma_f32_32x32x16_bf16 %[ag0], v[176:179], v[208:211], %[ag0]\n\t"
      "v_rcp_f32 v165, v165\n\t"
      "v_rcp_f32 v166, v166\n\t"
      "v_rcp_f32 v167, v167\n\t"
      "v_rcp_f32 v168, v168\n\t"
      "v_mfma_f32_32x32x16_bf16 %[ag0], v[180:183], v[212:215], %[ag0]\n\t"
      "v_rcp_f32 v169, v169\n\t"
      "v_rcp_f32 v170, v170\n\t"
      "v_rcp_f32 v171, v171\n\t"
      "v_rcp_f32 v172, v172\n\t"
      "v_rcp_f32 v173, v173\n\t"
      "v_rcp_f32 v174, v174\n\t"
      "v_rcp_f32 v175, v175\n\t"
      "v_pk_mul_f32 v[160:161], v[144:145], v[160:161]\n\t"
      "v_pk_mul_f32 v[162:163], v[146:147], v[162:163]\n\t"
      "v_pk_mul_f32 v[164:165], v[148:149], v[164:165]\n\t"
      "v_pk_mul_f32 v[166:167], v[150:151], v[166:167]\n\t"
      "v_pk_mul_f32 v[168:169], v[152:153], v[168:169]\n\t"
      "v_pk_mul_f32 v[170:171], v[154:155], v[170:171]\n\t"
      "v_pk_mul_f32 v[172:173], v[156:157], v[172:173]\n\t"
      "v_pk_mul_f32 v[174:175], v[158:159], v[174:175]\n\t"
      "v_cvt_pk_bf16_f32 v176, v160, v161\n\t"
      "v_cvt_pk_bf16_f32 v177, v162, v163\n\t"
      "v_cvt_pk_bf16_f32 v178, v164, v165\n\t"
      "v_cvt_pk_bf16_f32 v179, v166, v167\n\t"
      "v_cvt_pk_bf16_f32 v180, v168, v169\n\t"
      "v_cvt_pk_bf16_f32 v181, v170, v171\n\t"
      "v_cvt_pk_bf16_f32 v182, v172, v173\n\t"
      "v_cvt_pk_bf16_f32 v183, v174, v175\n\t"
      "s_sub_u32 %[t], %[t], 1\n\t"
      "s_cmp_eq_u32 %[t], 0\n\t"
      "s_cbranch_scc1 Lexit_even_%=\n\t"
      "v_exp_f32 v160, v128\n\t"
      "v_mfma_f32_32x32x16_bf16 v[144:159], v[200:203], %[q10], 0\n\t"
      "v_exp_f32 v161, v129\n\t"
      "v_exp_f32 v162, v130\n\t"
      "v_exp_f32 v163, v131\n\t"
      "v_exp_f32 v164, v132\n\t"
      "v_mfma_f32_32x32x16_bf16 v[144:159], v[204:207], %[q11], v[144:159]\n\t"
      "v_exp_f32 v165, v133\n\t"
      "s_cmp_lt_i32 %[k], 0\n\t"
      "s_cbranch_scc1 Lw87_%=\n\t"
      "s_waitcnt vmcnt(4)\n\t"
      "Lw87_%=:\n\t"
      "s_waitcnt vmcnt(8)\n\t"
      "ds_read_b128 v[200:203], %[sa0] offset:4096\n\t"
      "ds_read_b128 v[204:207], %[sa1] offset:4096\n\t"
      "ds_read_b128 v[184:187], %[pa0] offset:0\n\t"
      "ds_read_b128 v[188:191], %[pa1] offset:0\n\t"
      "v_exp_f32 v166, v134\n\t"
      "v_exp_f32 v167, v135\n\t"
      "v_exp_f32 v168, v136\n\t"
      "v_mfma_f32_32x32x16_bf16 v[144:159], v[192:195], %[id0], v[144:159]\n\t"
      "v_exp_f32 v169, v137\n\t"
      "v_exp_f32 v170, v138\n\t"
      "v_exp_f32 v171, v139\n\t"
      "v_exp_f32 v172, v140\n\t"
      "v_exp_f32 v173, v141\n\t"
      "v_exp_f32 v174, v142\n\t"
      "v_exp_f32 v175, v143\n\t"
      "v_pk_add_f32 v[160:161], v[160:161], 1.0 op_sel_hi:[1,0]\n\t"
      "v_pk_add_f32 v[162:163], v[162:163], 1.0 op_sel_hi:[1,0]\n\t"
      "v_pk_add_f32 v[164:165], v[164:165], 1.0 op_sel_hi:[1,0]\n\t"
      "v_pk_add_f32 v[166:167], v[166:167], 1.0 op_sel_hi:[1,0]\n\t"
      "v_pk_add_f32 v[168:169], v[168:169], 1.0 op_sel_hi:[1,0]\n\t"
      "v_pk_add_f32 v[170:171], v[170:171], 1.0 op_sel_hi:[1,0]\n\t"
      "v_pk_add_f32 v[172:173], v[172:173], 1.0 op_sel_hi:[1,0]\n\t"
      "v_pk_add_f32 v[174:175], v[174:175], 1.0 op_sel_hi:[1,0]\n\t"
      "v_rcp_f32 v160, v160\n\t"
      "v_mfma_f32_32x32x16_bf16 v[144:159], v[196:199], %[id1], v[144:159]\n\t"
      "v_rcp_f32 v161, v161\n\t"
      "ds_read_b128 v[192:195], %[pa2] offset:0\n\t"
      "ds_read_b128 v[196:199], %[pa3] offset:0\n\t"
      "ds_read_b32 v216, %[lst] offset:512\n\t"
      "ds_read_b32 v218, %[lst] offset:544\n\t"
      "ds_read_b32 v220, %[lst] offset:576\n\t"
      "ds_read_b32 v222, %[lst] offset:608\n\t"
      "v_rcp_f32 v162, v162\n\t"
      "v_rcp_f32 v163, v163\n\t"
      "v_rcp_f32 v164, v164\n\t"
      "v_mfma_f32_32x32x16_bf16 %[ag1], v[176:179], v[208:211], %[ag1]\n\t"
      "v_rcp_f32 v165, v165\n\t"
      "v_rcp_f32 v166, v166\n\t"
      "v_rcp_f32 v167, v167\n\t"
      "v_rcp_f32 v168, v168\n\t"
      "v_mfma_f32_32x32x16_bf16 %[ag1], v[180:183], v[212:215], %[ag1]\n\t"
      "v_rcp_f32 v169, v169\n\t"
      "ds_read_b64_tr_b16 v[208:209], %[st0] offset:2048\n\t"
      "ds_read_b64_tr_b16 v[210:211], %[st1] offset:2048\n\t"
      "ds_read_b64_tr_b16 v[212:213], %[st0] offset:3072\n\t"
      "ds_read_b64_tr_b16 v[214:215], %[st1] offset:3072\n\t"
      "v_rcp_f32 v170, v170\n\t"
      "v_rcp_f32 v171, v171\n\t"
      "v_rcp_f32 v172, v172\n\t"
      "v_rcp_f32 v173, v173\n\t"
      "v_rcp_f32 v174, v174\n\t"
      "v_rcp_f32 v175, v175\n\t"
      "v_pk_mul_f32 v[160:161], v[128:129], v[160:161]\n\t"
      "v_pk_mul_f32 v[162:163], v[130:131], v[162:163]\n\t"
      "v_pk_mul_f32 v[164:165], v[132:133], v[164:165]\n\t"
      "v_pk_mul_f32 v[166:167], v[134:135], v[166:167]\n\t"
      "v_pk_mul_f32 v[168:169], v[136:137], v[168:169]\n\t"
      "v_pk_mul_f32 v[170:171], v[138:139], v[170:171]\n\t"
      "v_pk_mul_f32 v[172:173], v[140:141], v[172:173]\n\t"
      "v_pk_mul_f32 v[174:175], v[142:143], v[174:175]\n\t"
      "v_cvt_pk_bf16_f32 v176, v160, v161\n\t"
      "v_cvt_pk_bf16_f32 v177, v162, v163\n\t"
      "v_cvt_pk_bf16_f32 v178, v164, v165\n\t"
      "v_cvt_pk_bf16_f32 v179, v166, v167\n\t"
      "v_cvt_pk_bf16_f32 v180, v168, v169\n\t"
      "v_cvt_pk_bf16_f32 v181, v170, v171\n\t"
      "v_cvt_pk_bf16_f32 v182, v172, v173\n\t"
      "v_cvt_pk_bf16_f32 v183, v174, v175\n\t"
      "s_waitcnt lgkmcnt(4)\n\t"
      "s_cmp_lt_i32 %[k], 1\n\t"
      "s_cbranch_scc1 Lnd8_%=\n\t"
      "s_add_u32 m0, %[ring], 0\n\t"
      "s_nop 0\n\t"
      "buffer_load_dwordx4 v[216:217], %[srd], 0 idxen offen lds\n\t"
      "s_add_u32 m0, %[ring], 1024\n\t"
      "s_nop 0\n\t"
      "buffer_load_dwordx4 v[218:219], %[srd], 0 idxen offen lds\n\t"
      "s_add_u32 m0, %[ring], 2048\n\t"
      "s_nop 0\n\t"
      "buffer_load_dwordx4 v[220:221], %[srd], 0 idxen offen lds\n\t"
      "s_add_u32 m0, %[ring], 3072\n\t"
      "s_nop 0\n\t"
      "buffer_load_dwordx4 v[222:223], %[srd], 0 idxen offen lds\n\t"
      "s_branch Lge8_%=\n\t"
      "Lnd8_%=:\n\t"
      "s_cmp_lg_i32 %[k], 0\n\t"
      "s_cbranch_scc1 Lge8_%=\n\t"
      "global_load_dwordx4 v[224:227], %[wuo], %[wub] offset:-4096\n\t"
      "global_load_dwordx4 v[228:231], %[wuo], %[wub] offset:-3072\n\t"
      "global_load_dwordx4 v[232:235], %[wuo], %[wub] offset:-2048\n\t"
      "global_load_dwordx4 v[236:239], %[wuo], %[wub] offset:-1024\n\t"
      "global_load_dwordx4 v[240:243], %[wuo], %[wub] offset:0\n\t"
      "global_load_dwordx4 v[244:247], %[wuo], %[wub] offset:1024\n\t"
      "global_load_dwordx4 v[248:251], %[wuo], %[wub] offset:2048\n\t"
      "global_load_dwordx4 v[252:255], %[wuo], %[wub] offset:3072\n\t"
      "Lge8_%=:\n\t"
      "s_sub_i32 %[k], %[k], 1\n\t"
      "v_exp_f32 v160, v144\n\t"
      "v_mfma_f32_32x32x16_bf16 v[128:143], v[200:203], %[q00], 0\n\t"
      "v_exp_f32 v161, v145\n\t"
      "v_exp_f32 v162, v146\n\t"
      "v_exp_f32 v163, v147\n\t"
      "v_exp_f32 v164, v148\n\t"
      "v_mfma_f32_32x32x16_bf16 v[128:143], v[204:207], %[q01], v[128:143]\n\t"
      "v_exp_f32 v165, v149\n\t"
      "v_exp_f32 v166, v150\n\t"
      "v_exp_f32 v167, v151\n\t"
      "v_exp_f32 v168, v152\n\t"
      "v_mfma_f32_32x32x16_bf16 v[128:143], v[184:187], %[id0], v[128:143]\n\t"
      "v_exp_f32 v169, v153\n\t"
      "v_exp_f32 v170, v154\n\t"
      "v_exp_f32 v171, v155\n\t"
      "v_exp_f32 v172, v156\n\t"
      "v_exp_f32 v173, v157\n\t"
      "v_exp_f32 v174, v158\n\t"
      "v_exp_f32 v175, v159\n\t"
      "v_pk_add_f32 v[160:161], v[160:161], 1.0 op_sel_hi:[1,0]\n\t"
      "v_pk_add_f32 v[162:163], v[162:163], 1.0 op_sel_hi:[1,0]\n\t"
      "v_pk_add_f32 v[164:165], v[164:165], 1.0 op_sel_hi:[1,0]\n\t"
      "v_pk_add_f32 v[166:167], v[166:167], 1.0 op_sel_hi:[1,0]\n\t"
      "v_pk_add_f32 v[168:169], v[168:169], 1.0 op_sel_hi:[1,0]\n\t"
      "v_pk_add_f32 v[170:171], v[170:171], 1.0 op_sel_hi:[1,0]\n\t"
      "v_pk_add_f32 v[172:173], v[172:173], 1.0 op_sel_hi:[1,0]\n\t"
      "v_pk_add_f32 v[174:175], v[174:175], 1.0 op_sel_hi:[1,0]\n\t"
      "v_rcp_f32 v160, v160\n\t"
      "v_mfma_f32_32x32x16_bf16 v[128:143], v[188:191], %[id1], v[128:143]\n\t"
      "v_rcp_f32 v161, v161\n\t"
      "s_waitcnt lgkmcnt(0)\n\t"
      "v_rcp_f32 v162, v162\n\t"
      "v_rcp_f32 v163, v163\n\t"
      "v_rcp_f32 v164, v164\n\t"
      "v_mfma_f32_32x32x16_bf16 %[ag0], v[176:179], v[208:211], %[ag0]\n\t"
      "v_rcp_f32 v165, v165\n\t"
      "v_rcp_f32 v166, v166\n\t"
      "v_rcp_f32 v167, v167\n\t"
      "v_rcp_f32 v168, v168\n\t"
      "v_mfma_f32_32x32x16_bf16 %[ag0], v[180:183], v[212:215], %[ag0]\n\t"
      "v_rcp_f32 v169, v169\n\t"
      "v_rcp_f32 v170, v170\n\t"
      "v_rcp_f32 v171, v171\n\t"
      "v_rcp_f32 v172, v172\n\t"
      "v_rcp_f32 v173, v173\n\t"
      "v_rcp_f32 v174, v174\n\t"
      "v_rcp_f32 v175, v175\n\t"
      "v_pk_mul_f32 v[160:161], v[144:145], v[160:161]\n\t"
      "v_pk_mul_f32 v[162:163], v[146:147], v[162:163]\n\t"
      "v_pk_mul_f32 v[164:165], v[148:149], v[164:165]\n\t"
      "v_pk_mul_f32 v[166:167], v[150:151], v[166:167]\n\t"
      "v_pk_mul_f32 v[168:169], v[152:153], v[168:169]\n\t"
      "v_pk_mul_f32 v[170:171], v[154:155], v[170:171]\n\t"
      "v_pk_mul_f32 v[172:173], v[156:157], v[172:173]\n\t"
      "v_pk_mul_f32 v[174:175], v[158:159], v[174:175]\n\t"
      "v_cvt_pk_bf16_f32 v176, v160, v161\n\t"
      "v_cvt_pk_bf16_f32 v177, v162, v163\n\t"
      "v_cvt_pk_bf16_f32 v178, v164, v165\n\t"
      "v_cvt_pk_bf16_f32 v179, v166, v167\n\t"
      "v_cvt_pk_bf16_f32 v180, v168, v169\n\t"
      "v_cvt_pk_bf16_f32 v181, v170, v171\n\t"
      "v_cvt_pk_bf16_f32 v182, v172, v173\n\t"
      "v_cvt_pk_bf16_f32 v183, v174, v175\n\t"
      "v_add_u32 %[sa0], 0x1000, %[sa0]\n\t"
      "v_add_u32 %[sa1], 0x1000, %[sa1]\n\t"
      "v_add_u32 %[st0], 0x1000, %[st0]\n\t"
      "v_add_u32 %[st1], 0x1000, %[st1]\n\t"
      "v_add_u32 %[lst], 0x100, %[lst]\n\t"
      "s_sub_u32 %[t], %[t], 1\n\t"
      "s_cmp_lg_u32 %[t], 0\n\t"
      "s_cbranch_scc1 Lloop_%=\n\t"
      "s_branch Ltail_%=\n\t"
      "Lexit_even_%=:\n\t"
      "v_add_u32 %[st0], 0x800, %[st0]\n\t"
      "v_add_u32 %[st1], 0x800, %[st1]\n\t"
      "Ltail_%=:\n\t"
      "s_nop 1\n\t"
      "v_mfma_f32_32x32x16_bf16 %[ag1], v[176:179], v[208:211], %[ag1]\n\t"
      "v_mfma_f32_32x32x16_bf16 %[ag1], v[180:183], v[212:215], %[ag1]\n\t"
      "s_cmp_eq_u32 %[m], 0\n\t"
      "s_cbranch_scc1 Ldone_%=\n\t"
      "ds_read_b64_tr_b16 v[208:209], %[st0] offset:0\n\t"
      "ds_read_b64_tr_b16 v[210:211], %[st1] offset:0\n\t"
      "ds_read_b64_tr_b16 v[212:213], %[st0] offset:1024\n\t"
      "ds_read_b64_tr_b16 v[214:215], %[st1] offset:1024\n\t"
      "v_mfma_f32_32x32x16_bf16 v[144:159], v[200:203], %[q10], 0\n\t"
      "v_mfma_f32_32x32x16_bf16 v[144:159], v[204:207], %[q11], v[144:159]\n\t"
      "v_mov_b32 v178, 0\n\t"
      "v_mov_b32 v179, 0\n\t"
      "v_mov_b32 v180, 0\n\t"
      "v_mov_b32 v181, 0\n\t"
      "v_mov_b32 v182, 0\n\t"
      "v_mov_b32 v183, 0\n\t"
      "v_mfma_f32_32x32x16_bf16 v[144:159], v[192:195], %[id0], v[144:159]\n\t"
      "v_mfma_f32_32x32x16_bf16 v[144:159], v[196:199], %[id1], v[144:159]\n\t"
      "v_exp_f32 v160, v128\n\t"
      "v_exp_f32 v161, v129\n\t"
      "v_exp_f32 v162, v130\n\t"
      "v_exp_f32 v163, v131\n\t"
      "s_nop 0\n\t"
      "v_add_f32 v160, 1.0, v160\n\t"
      "v_add_f32 v161, 1.0, v161\n\t"
      "v_add_f32 v162, 1.0, v162\n\t"
      "v_add_f32 v163, 1.0, v163\n\t"
      "v_rcp_f32 v160, v160\n\t"
      "v_rcp_f32 v161, v161\n\t"
      "v_rcp_f32 v162, v162\n\t"
      "v_rcp_f32 v163, v163\n\t"
      "s_nop 0\n\t"
      "v_mul_f32 v160, v128, v160\n\t"
      "v_mul_f32 v161, v129, v161\n\t"
      "v_mul_f32 v162, v130, v162\n\t"
      "v_mul_f32 v163, v131, v163\n\t"
      "v_cvt_pk_bf16_f32 v176, v160, v161\n\t"
      "v_cvt_pk_bf16_f32 v177, v162, v163\n\t"
      "s_cmp_lt_u32 %[m], 2\n\t"
      "s_cbranch_scc1 Lqa_%=\n\t"
      "v_exp_f32 v164, v132\n\t"
      "v_exp_f32 v165, v133\n\t"
      "v_exp_f32 v166, v134\n\t"
      "v_exp_f32 v167, v135\n\t"
      "s_nop 0\n\t"
      "v_add_f32 v164, 1.0, v164\n\t"
      "v_add_f32 v165, 1.0, v165\n\t"
      "v_add_f32 v166, 1.0, v166\n\t"
      "v_add_f32 v167, 1.0, v167\n\t"
      "v_rcp_f32 v164, v164\n\t"
      "v_rcp_f32 v165, v165\n\t"
      "v_rcp_f32 v166, v166\n\t"
      "v_rcp_f32 v167, v167\n\t"
      "s_nop 0\n\t"
      "v_mul_f32 v164, v132, v164\n\t"
      "v_mul_f32 v165, v133, v165\n\t"
      "v_mul_f32 v166, v134, v166\n\t"
      "v_mul_f32 v167, v135, v167\n\t"
      "v_cvt_pk_bf16_f32 v178, v164, v165\n\t"
      "v_cvt_pk_bf16_f32 v179, v166, v167\n\t"
      "s_cmp_lt_u32 %[m], 3\n\t"
      "s_cbranch_scc1 Lqa_%=\n\t"
      "v_exp_f32 v168, v136\n\t"
      "v_exp_f32 v169, v137\n\t"
      "v_exp_f32 v170, v138\n\t"
      "v_exp_f32 v171, v139\n\t"
      "s_nop 0\n\t"
      "v_add_f32 v168, 1.0, v168\n\t"
      "v_add_f32 v169, 1.0, v169\n\t"
      "v_add_f32 v170, 1.0, v170\n\t"
      "v_add_f32 v171, 1.0, v171\n\t"
      "v_rcp_f32 v168, v168\n\t"
      "v_rcp_f32 v169, v169\n\t"
      "v_rcp_f32 v170, v170\n\t"
      "v_rcp_f32 v171, v171\n\t"
      "s_nop 0\n\t"
      "v_mul_f32 v168, v136, v168\n\t"
      "v_mul_f32 v169, v137, v169\n\t"
      "v_mul_f32 v170, v138, v170\n\t"
      "v_mul_f32 v171, v139, v171\n\t"
      "v_cvt_pk_bf16_f32 v180, v168, v169\n\t"
      "v_cvt_pk_bf16_f32 v181, v170, v171\n\t"
      "Lqa_%=:\n\t"
      "s_waitcnt lgkmcnt(0)\n\t"
      "s_nop 1\n\t"
      "v_mfma_f32_32x32x16_bf16 %[ag0], v[176:179], v[208:211], %[ag0]\n\t"
      "v_mfma_f32_32x32x16_bf16 %[ag0], v[180:183], v[212:215], %[ag0]\n\t"
      "v_exp_f32 v160, v144\n\t"
      "v_exp_f32 v161, v145\n\t"
      "v_exp_f32 v162, v146\n\t"
      "v_exp_f32 v163, v147\n\t"
      "s_nop 0\n\t"
      "v_add_f32 v160, 1.0, v160\n\t"
      "v_add_f32 v161, 1.0, v161\n\t"
      "v_add_f32 v162, 1.0, v162\n\t"
      "v_add_f32 v163, 1.0, v163\n\t"
      "v_rcp_f32 v160, v160\n\t"
      "v_rcp_f32 v161, v161\n\t"
      "v_rcp_f32 v162, v162\n\t"
      "v_rcp_f32 v163, v163\n\t"
      "s_nop 0\n\t"
      "v_mul_f32 v160, v144, v160\n\t"
      "v_mul_f32 v161, v145, v161\n\t"
      "v_mul_f32 v162, v146, v162\n\t"
      "v_mul_f32 v163, v147, v163\n\t"
      "v_cvt_pk_bf16_f32 v176, v160, v161\n\t"
      "v_cvt_pk_bf16_f32 v177, v162, v163\n\t"
      "s_cmp_lt_u32 %[m], 2\n\t"
      "s_cbranch_scc1 Lqb_%=\n\t"
      "v_exp_f32 v164, v148\n\t"
      "v_exp_f32 v165, v149\n\t"
      "v_exp_f32 v166, v150\n\t"
      "v_exp_f32 v167, v151\n\t"
      "s_nop 0\n\t"
      "v_add_f32 v164, 1.0, v164\n\t"
      "v_add_f32 v165, 1.0, v165\n\t"
      "v_add_f32 v166, 1.0, v166\n\t"
      "v_add_f32 v167, 1.0, v167\n\t"
      "v_rcp_f32 v164, v164\n\t"
      "v_rcp_f32 v165, v165\n\t"
      "v_rcp_f32 v166, v166\n\t"
      "v_rcp_f32 v167, v167\n\t"
      "s_nop 0\n\t"
      "v_mul_f32 v164, v148, v164\n\t"
      "v_mul_f32 v165, v149, v165\n\t"
      "v_mul_f32 v166, v150, v166\n\t"
      "v_mul_f32 v167, v151, v167\n\t"
      "v_cvt_pk_bf16_f32 v178, v164, v165\n\t"
      "v_cvt_pk_bf16_f32 v179, v166, v167\n\t"
      "s_cmp_lt_u32 %[m], 3\n\t"
      "s_cbranch_scc1 Lqb_%=\n\t"
      "v_exp_f32 v168, v152\n\t"
      "v_exp_f32 v169, v153\n\t"
      "v_exp_f32 v170, v154\n\t"
      "v_exp_f32 v171, v155\n\t"
      "s_nop 0\n\t"
      "v_add_f32 v168, 1.0, v168\n\t"
      "v_add_f32 v169, 1.0, v169\n\t"
      "v_add_f32 v170, 1.0, v170\n\t"
      "v_add_f32 v171, 1.0, v171\n\t"
      "v_rcp_f32 v168, v168\n\t"
      "v_rcp_f32 v169, v169\n\t"
      "v_rcp_f32 v170, v170\n\t"
      "v_rcp_f32 v171, v171\n\t"
      "s_nop 0\n\t"
      "v_mul_f32 v168, v152, v168\n\t"
      "v_mul_f32 v169, v153, v169\n\t"
      "v_mul_f32 v170, v154, v170\n\t"
      "v_mul_f32 v171, v155, v171\n\t"
      "v_cvt_pk_bf16_f32 v180, v168, v169\n\t"
      "v_cvt_pk_bf16_f32 v181, v170, v171\n\t"
      "Lqb_%=:\n\t"
      "s_nop 1\n\t"
      "v_mfma_f32_32x32x16_bf16 %[ag1], v[176:179], v[208:211], %[ag1]\n\t"
      "v_mfma_f32_32x32x16_bf16 %[ag1], v[180:183], v[212:215], %[ag1]\n\t"
      "Ldone_%=:\n\t"
      "s_mov_b32 m0, %[keep]\n\t"
      "s_waitcnt vmcnt(0) lgkmcnt(0)\n\t"
      "s_nop 11\n\t"
      : [ag0] "+v"(agg0), [ag1] "+v"(agg1), "=&{v[224:239]}"(wu0), "=&{v[240:255]}"(wu1), [sa0] "+v"(sa0), [sa1] "+v"(sa1),
        [st0] "+v"(st0), [st1] "+v"(st1), [lst] "+v"(lst), [k] "+s"(k), [t] "=&s"(t), [keep] "=&s"(keep)
      : [q00] "v"(q00), [q01] "v"(q01), [q10] "v"(q10), [q11] "v"(q11), [id0] "v"(id0), [id1] "v"(id1), [pa0] "v"(pa0),
        [pa1] "v"(pa0 ^ 32u), [pa2] "v"(pa0 ^ 64u), [pa3] "v"(pa0 ^ 96u), [s0] "v"(s0), [s1] "v"(s0 ^ 64u), [wuo] "v"(wuo),
        [srd] "s"(srd), [wub] "s"(wub), [ring] "s"(ring), [nf] "s"(nf), [m] "s"(m)
      : "memory", "scc", "v128", "v129", "v130", "v131", "v132", "v133", "v134", "v135", "v136", "v137", "v138", "v139", "v140", "v141", "v142", "v143", "v144", "v145", "v146", "v147", "v148", "v149", "v150", "v151", "v152", "v153", "v154", "v155", "v156", "v157", "v158", "v159", "v160", "v161", "v162", "v163", "v164", "v165", "v166", "v167", "v168", "v169", "v170", "v171", "v172", "v173", "v174", "v175", "v176", "v177", "v178", "v179", "v180", "v181", "v182", "v183", "v184", "v185", "v186", "v187", "v188", "v189", "v190", "v191", "v192", "v193", "v194", "v195", "v196", "v197", "v198", "v199", "v200", "v201", "v202", "v203", "v204", "v205", "v206", "v207", "v208", "v209", "v210", "v211", "v212", "v213", "v214", "v215", "v216", "v217", "v218", "v219", "v220", "v221", "v222", "v223");
}

DI void put_record(char* lds, int sub, unsigned row, Rec r) {
  const unsigned rr = sub * CAP + row, sw = (rr >> 2) & 3u, k = r.a & 255u;
  char* srow = lds + L_S + rr * 64;
  *(unsigned*)(lds + L_LIST + rr * 4) = r.a >> 8;
  *(unsigned short*)(srow + (((k >> 3) ^ sw) << 4) + ((k & 7u) << 1)) = 0x3F80;
  u32x4 e = {r.e01, r.e23, r.e01, r.e23};
  *(u32x4*)(srow + ((3u ^ sw) << 4)) = e;
}

DI void clear_images(char* lds, int tid) {
  const u32x4 z = {0, 0, 0, 0}, o = {OOB, OOB, OOB, OOB};
#pragma unroll
  for (int i = 0; i < (L_LIST / 16 + 511) / 512; ++i)
    if (tid + i * 512 < L_LIST / 16) *(u32x4*)(lds + (tid + i * 512) * 16) = z;
  if (tid < (L_ZEND - L_LIST) / 16) *(u32x4*)(lds + L_LIST + tid * 16) = o;
}

__global__ __launch_bounds__(512, 2) void mpnn_edge(const unsigned short* __restrict__ x, const char* __restrict__ ws,
                                                    unsigned short* __restrict__ out) {
  __shared__ __attribute__((aligned(16))) char lds[L_TOTAL];
  const int tid = threadIdx.x, lane = tid & 63, w = __builtin_amdgcn_readfirstlane(tid >> 6);
  const int g = blockIdx.x, ord = (g & 7) * 32 + (g >> 3);
  const int sub = w >> 2, fh = w & 1, half = (w >> 1) & 1, w4 = w & 3;
  const int c = lane & 31, h = lane >> 5, pc = w4 * 16 + (lane >> 2);
  STAMP_DECL
  STAMP(0);

  const u32x2 tb = *(const u32x2*)(ws + WS_TAB + pc * 2048 + ord * 8);
  const unsigned ring = lds_addr(lds) + L_RING + w * 8192;
  {
    const u32x4 xs = make_srd(x, NN * 256), ws2 = make_srd(ws + WS_W2, 32768);
    const unsigned r4 = lane >> 4, c16 = lane & 15;
    unsigned row = w * 4 + r4;
    dma16(xs, (row * 256 + g) * 256 + ((c16 ^ (row & 15)) << 4), lds_addr(lds) + L_X + w * 1024);
    if (w < 4) {
      row += 32;
      dma16(xs, (row * 256 + g) * 256 + ((c16 ^ (row & 15)) << 4), lds_addr(lds) + L_X + (w + 8) * 1024);
    }
#pragma unroll
    for (int i = 0; i < 4; ++i) dma16(ws2, (w * 4 + i) * 1024 + lane * 16, ring + 4096 + i * 1024);
  }
  const int fbq = 2 * fh + half;
  const u32x4 w3 = *(const u32x4*)(ws + WS_W3 + (fbq * 32 + c) * 16);
  clear_images(lds, tid);
  STAMP(1);

  const unsigned sA = tb[0] & 0xffffu, sB = tb[0] >> 16, eB = tb[1];
  const unsigned first = sub ? sB : sA, cnt = sub ? eB - sB : sB - sA, l4 = lane & 3;
  const Rec* rp = (const Rec*)(ws + WS_REC) + pc * CHUNK + first;
  Rec rec[NREC];
#pragma unroll
  for (int i = 0; i < NREC; ++i) {
    rec[i] = Rec{0, 0, 0};
    if (l4 + 4 * i < cnt) rec[i] = rp[l4 + 4 * i];
  }
  const unsigned incl = wave_scan(l4 == 0 ? cnt : 0u);
  unsigned* wsum = (unsigned*)(lds + L_WSUM);
  if (lane == 63) wsum[w] = incl;
  STAMP(2);
  barrier_all();
  STAMP(3);
  const u32x4 wa = *(const u32x4*)wsum, wb = *(const u32x4*)(wsum + 4);
  const unsigned nA = wa[0] + wa[1] + wa[2] + wa[3], nB = wb[0] + wb[1] + wb[2] + wb[3];
  const u32x4 wm = sub ? wb : wa;
  const unsigned pos = (w4 > 0 ? wm[0] : 0) + (w4 > 1 ? wm[1] : 0) + (w4 > 2 ? wm[2] : 0) + incl - cnt;
  const int ntot = __builtin_amdgcn_readfirstlane(sub ? nB : nA);
  const int nmax = __builtin_amdgcn_readfirstlane(max(nA, nB));
#pragma unroll
  for (int i = 0; i < NREC; ++i)
    if (l4 + 4 * i < cnt && pos + l4 + 4 * i < CAP) put_record(lds, sub, pos + l4 + 4 * i, rec[i]);
  for (unsigned i = l4 + 4 * NREC; i < cnt && pos + i < CAP; i += 4) put_record(lds, sub, pos + i, rp[i]);

  bf16x8 qf[2][2];
  {
    const int xrow = sub * SUBN + min(c, SUBN - 1);
    const char* wq = lds + L_RING + 4096 + fbq * 16384 + lane * 16;
    f32x16 q = {};
#pragma unroll
    for (int s = 0; s < 8; ++s)
      q = mfma32(lds_frag(lds + L_X + 256 * xrow + 16 * ((2 * s + h) ^ (xrow & 15))),
                 lds_frag(wq + (s >> 2) * 8192 + (s & 3) * 1024), q);
    u32x4 a, b;
#pragma unroll
    for (int i = 0; i < 4; ++i) {
      a[i] = pack2(q[2 * i] * NEG_LOG2E, q[2 * i + 1] * NEG_LOG2E);
      b[i] = pack2(q[8 + 2 * i] * NEG_LOG2E, q[9 + 2 * i] * NEG_LOG2E);
      b[i] = h ? w3[i] : b[i];
    }
    char* xq = lds + L_RING + (w ^ 2) * 8192 + lane * 16;
    *(u32x4*)xq = a;
    *(u32x4*)(xq + 1024) = b;
    STAMP(4);
    barrier_lds();
    STAMP(5);
    const char* rq = lds + L_RING + w * 8192 + lane * 16;
    const u32x4 oa = *(const u32x4*)rq, ob = *(const u32x4*)(rq + 1024);
    qf[0][0] = as_frag(half ? oa : a);
    qf[0][1] = as_frag(half ? ob : b);
    qf[1][0] = as_frag(half ? a : oa);
    qf[1][1] = as_frag(half ? b : ob);
    asm volatile("s_waitcnt lgkmcnt(0)" : "+v"(qf[0][0]), "+v"(qf[0][1]), "+v"(qf[1][0]), "+v"(qf[1][1])::"memory");
  }

  bf16x8 id[2];
#pragma unroll
  for (int s = 0; s < 2; ++s) {
    u32x4 v;
#pragma unroll
    for (int i = 0; i < 4; ++i) {
      const int f0 = ((2 * i) & 3) + 16 * h + 8 * (i >> 1) + 4 * s;
      v[i] = (c == f0 ? 0x3F80u : 0u) | (c == f0 + 1 ? 0x3F800000u : 0u);
    }
    id[s] = as_frag(v);
  }

  u32x4 srd = make_srd(ws + WS_P, P_BYTES / 256);
  srd[1] |= 256u << 16;
  const unsigned s0 = fh * 128 + (((lane & 7) ^ (lane >> 4)) << 4);
  const unsigned pa0 = ring + c * 128 + ((h ^ ((c >> 1) & 7)) << 4);
  const unsigned lb = lds_addr(lds);
  u32x16 wu0, wu1;

  f32x16 agg0 = {}, agg1 = {};
  for (int base = 0;;) {
    const int nw = min(max(ntot - base, 0), CAP), oct = (nw + 7) >> 3;
    const int o0 = sub ? oct >> 1 : (oct + 1) >> 1;
    const int myo = half ? oct - o0 : o0, r0 = sub * CAP + (half ? o0 * 8 : 0);
    const int sr = r0 + c;
    const unsigned sa0 = lb + L_S + sr * 64 + ((h ^ ((sr >> 2) & 3)) << 4);
    const int tq = (lane & 15) >> 2, tp = ((lane & 1) << 1) | ((lane >> 1) & 1), tch = 2 * ((lane >> 4) & 1) + (tp >> 1);
    const int tr0 = r0 + 4 * h + tq, tr1 = tr0 + 8;
    const unsigned st0 = lb + L_S + tr0 * 64 + 16 * (tch ^ ((tr0 >> 2) & 3)) + 8 * (tp & 1);
    const unsigned st1 = lb + L_S + tr1 * 64 + 16 * (tch ^ ((tr1 >> 2) & 3)) + 8 * (tp & 1);
    STAMP(6);
    edge_tiles(agg0, agg1, wu0, wu1, qf[0][0], qf[0][1], qf[1][0], qf[1][1], id[0], id[1], pa0, sa0, sa0 ^ 32u, st0, st1,
               lb + L_LIST + (r0 + (lane >> 3)) * 4, s0, w * 8192 + lane * 16, srd, ws + WS_WU + 4096, ring, myo >> 2, myo & 3);
    base += CAP;
    if (base >= nmax) break;
    barrier_all();
    clear_images(lds, tid);
    barrier_lds();
    const unsigned skip = pos < (unsigned)base ? (base - pos) & ~3u : 0u;
    for (unsigned i = skip + l4; i < cnt && pos + i < (unsigned)(base + CAP); i += 4)
      if (pos + i >= (unsigned)base) put_record(lds, sub, pos + i - base, rp[i]);
    barrier_all();
  }
  STAMP(8);

  const f32x16 give = half ? agg0 : agg1, mine = half ? agg1 : agg0;
#pragma unroll
  for (int q = 0; q < 4; ++q)
    *(f32x4*)(lds + L_RING + w * 8192 + q * 1024 + lane * 16) =
        (f32x4){give[4 * q], give[4 * q + 1], give[4 * q + 2], give[4 * q + 3]};
  const int n16 = lane & 15, kq = lane >> 4;
  f32x4 z[3] = {};
#pragma unroll
  for (int ct = 0; ct < 3; ++ct) {
#pragma unroll
    for (int s = 0; s < 4; ++s)
      z[ct] = mfma16(sub_frag(wu0, s), lds_frag(lds + L_X + 256 * (16 * ct + n16) + 16 * ((4 * s + kq) ^ n16)), z[ct]);
    z[ct] *= NEG_LOG2E;
  }
  barrier_lds();
  {
    const int row = sub * SUBN + c;
#pragma unroll
    for (int q = 0; q < 4; ++q) {
      const f32x4 o = *(const f32x4*)(lds + L_RING + (w ^ 2) * 8192 + q * 1024 + lane * 16);
      const u32x2 v = {pack2(mine[4 * q] + o[0], mine[4 * q + 1] + o[1]), pack2(mine[4 * q + 2] + o[2], mine[4 * q + 3] + o[3])};
      if (c < SUBN) *(u32x2*)(lds + L_AGG + 256 * row + 16 * ((4 * fbq + q) ^ (row & 15)) + 8 * h) = v;
    }
  }
  barrier_lds();
  STAMP(9);
#pragma unroll
  for (int ct = 0; ct < 3; ++ct) {
    const int row = min(16 * ct + n16, 2 * SUBN - 1);
#pragma unroll
    for (int s = 0; s < 4; ++s)
      z[ct] = mfma16(sub_frag(wu1, s), lds_frag(lds + L_AGG + 256 * row + 16 * ((4 * s + kq) ^ (row & 15))), z[ct]);
    float v[4];
#pragma unroll
    for (int i = 0; i < 4; ++i)
      v[i] = z[ct][i] * __builtin_amdgcn_rcpf(__builtin_fmaf(__builtin_amdgcn_exp2f(z[ct][i]), NEG_LOG2E, NEG_LOG2E));
    *(u32x2*)(lds + L_OUT + 256 * (16 * ct + n16) + 16 * ((2 * w + (kq >> 1)) ^ n16) + 8 * (kq & 1)) =
        (u32x2){pack2(v[0], v[1]), pack2(v[2], v[3])};
  }
  barrier_lds();
#pragma unroll
  for (int i = 0; i < 2; ++i) {
    const int k = tid + 512 * i, row = k >> 4, node = row * 256 + g;
    if (row < 2 * SUBN && node < NN)
      st16(out + (size_t)node * 128 + (k & 15) * 8, *(const u32x4*)(lds + L_OUT + 256 * row + 16 * ((k & 15) ^ (row & 15))));
  }
  STAMP(10);
  STAMPW(11);
  STAMP_OUT(1, STW);
}

}

extern "C" void kernel_launch(void* const* d_in, const int* in_sizes, int n_in, void* d_out, int out_size, void* d_ws,
                              size_t ws_size, hipStream_t stream) {
  const unsigned short* x = (const unsigned short*)d_in[0];
  mpnn_prep<<<K1_GRID, 512, 0, stream>>>(x, (const u32x2*)d_in[1], (const int*)d_in[2], (const int*)d_in[3],
                                         (const unsigned short*)d_in[4], (const unsigned short*)d_in[5], (char*)d_ws);
  mpnn_edge<<<256, 512, 0, stream>>>(x, (const char*)d_ws, (unsigned short*)d_out);
}
```

```cpp
#include <hip/hip_runtime.h>
#include <hip/hip_bf16.h>
#include <stdint.h>

namespace {

typedef __attribute__((ext_vector_type(2))) unsigned u32x2;
#define DI __device__ __forceinline__

constexpr int NN = 10000, NE = 160000;
constexpr int LDS_BYTES = 160000;

DI float bf2f(unsigned short u) { return __uint_as_float((unsigned)u << 16); }
DI float rbf(float v) { return (float)(__bf16)v; }
DI float silu(float v) { return v / (1.0f + __expf(-v)); }

DI void standalone(char* lds, const unsigned short* x, const unsigned short* eattr, const int* src, const int* dst,
                   const unsigned short* wmsg, const unsigned short* wupd, unsigned short* out) {
  unsigned short* w1 = (unsigned short*)lds;
  float* q = (float*)(lds + 32768);
  float* agg = q + 40 * 128;
  int* list = (int*)(agg + 40 * 128);
  int* cnt = list + 512;
  const int tid = threadIdx.x, f = tid & 127, slot = tid >> 7, g = (blockIdx.x & 7) * 32 + (blockIdx.x >> 3);
  for (int i = tid; i < 128 * 128; i += 512) w1[i] = wmsg[i];
  for (int j = slot; j < 40; j += 4) {
    const int node = j * 256 + g;
    float acc = 0.f;
    if (node < NN)
      for (int k = 0; k < 128; ++k) acc += bf2f(x[node * 128 + k]) * bf2f(wmsg[(128 + k) * 128 + f]);
    q[j * 128 + f] = acc;
    agg[j * 128 + f] = 0.f;
  }
  float w3[4];
#pragma unroll
  for (int k = 0; k < 4; ++k) w3[k] = bf2f(wmsg[(256 + k) * 128 + f]);
  for (int base = 0; base < NE; base += 512) {
    if (tid == 0) *cnt = 0;
    __syncthreads();
    const int e = base + tid;
    if (e < NE && (dst[e] & 255) == g) list[atomicAdd(cnt, 1)] = e;
    __syncthreads();
    const int n = *cnt;
    for (int i = slot; i < n; i += 4) {
      const int ed = list[i], j = dst[ed] >> 8;
      const unsigned short* xs = x + src[ed] * 128;
      float y = q[j * 128 + f];
      for (int k = 0; k < 128; ++k) y += bf2f(xs[k]) * bf2f(w1[k * 128 + f]);
#pragma unroll
      for (int k = 0; k < 4; ++k) y += bf2f(eattr[ed * 4 + k]) * w3[k];
      atomicAdd(&agg[j * 128 + f], rbf(silu(y)));
    }
    __syncthreads();
  }
  for (int j = slot; j < 40; j += 4) {
    const int node = j * 256 + g;
    if (node >= NN) continue;
    float z = 0.f;
    for (int k = 0; k < 128; ++k)
      z += bf2f(x[node * 128 + k]) * bf2f(wupd[k * 128 + f]) + rbf(agg[j * 128 + k]) * bf2f(wupd[(128 + k) * 128 + f]);
    out[node * 128 + f] = __builtin_bit_cast(unsigned short, (__bf16)silu(z));
  }
}

__global__ __launch_bounds__(512, 2) void mpnn_layer(const unsigned short* x, const unsigned short* eattr, const int* src,
                                                     const int* dst, const unsigned short* wmsg, const unsigned short* wupd,
                                                     char* ws, unsigned short* out) {
  __shared__ __attribute__((aligned(256))) char lds[LDS_BYTES];
  uint64_t ka = (uint64_t)__builtin_amdgcn_kernarg_segment_ptr();
  const unsigned b = blockIdx.x, tid = threadIdx.x;
  unsigned slow;
  asm volatile(
      "Lst_%=:\n\t"
      "s_load_dwordx16 s[12:27], %[ka], 0x0\n\t"
      "v_lshrrev_b32 v89, 6, v0\n\t"
      "s_mov_b32 s47, m0\n\t"
      "v_readfirstlane_b32 s48, v89\n\t"
      "s_cmp_gt_u32 s48, 3\n\t"
      "s_cbranch_scc1 Lbk_%=\n\t"
      "s_cmp_ge_u32 %[r], 128\n\t"
      "s_cbranch_scc1 Lfp_%=\n\t"
      "v_lshlrev_b32 v88, 2, v0\n\t"
      "v_mov_b32 v112, 0\n\t"
      "ds_write_b32 v88, v112 offset:43264\n\t"
      "s_movk_i32 s99, 250\n\t"
      "v_cmp_gt_u32 s[56:57], s99, v0\n\t"
      "s_movk_i32 s99, 232\n\t"
      "v_cmp_gt_u32 s[52:53], s99, v0\n\t"
      "s_mul_i32 s75, %[r], 5000\n\t"
      "s_lshl_b32 s76, s75, 1\n\t"
      "s_lshl_b32 s74, %[r], 11\n\t"
      "s_add_u32 s74, s74, 5136384\n\t"
      "s_and_b32 s78, %[b], 7\n\t"
      "s_lshl_b32 s78, s78, 5\n\t"
      "s_lshr_b32 s79, %[b], 3\n\t"
      "s_add_u32 s78, s78, s79\n\t"
      "s_lshl_b32 s92, s78, 8\n\t"
      "s_lshl_b32 s93, s78, 3\n\t"
      "s_add_u32 s93, s93, 5136384\n\t"
      "v_lshrrev_b32 v91, 1, v0\n\t"
      "v_lshlrev_b32 v94, 11, v91\n\t"
      "v_mul_u32_u24 v84, 20096, v91\n\t"
      "v_lshlrev_b32 v97, 4, v0\n\t"
      "v_and_b32 v82, 1, v0\n\t"
      "v_and_b32 v86, 31, v0\n\t"
      "v_bfe_u32 v87, v0, 5, 1\n\t"
      "v_lshrrev_b32 v93, 4, v0\n\t"
      "v_bfe_u32 v92, v0, 4, 2\n\t"
      "v_and_b32 v58, 63, v0\n\t"
      "v_lshlrev_b32 v58, 4, v58\n\t"
      "s_mov_b32 s98, 0\n\t"
      "s_mov_b32 s80, 0xbfb8aa3b\n\t"
      "s_mov_b32 s81, 0xbfb8aa3b\n\t"
      "v_mov_b32 v99, 32768\n\t"
      "v_mov_b32 v127, 1\n\t"
      "v_min_u32 v88, 249, v0\n\t"
      "v_mul_u32_u24 v88, 20, v88\n\t"
      "v_lshlrev_b32 v89, 1, v88\n\t"
      "s_movk_i32 s78, 205\n\t"
      "s_mov_b32 s79, -1310720\n\t"
      "v_mov_b32 v85, 1\n\t"
      "s_waitcnt lgkmcnt(0)\n\t"
      "s_add_u32 s70, s18, s75\n\t"
      "s_addc_u32 s71, s19, 0\n\t"
      "global_load_dwordx4 v[148:151], v88, s[70:71]\n\t"
      "global_load_dword v152, v88, s[70:71] offset:16\n\t"
      "s_add_u32 s72, s16, s75\n\t"
      "s_addc_u32 s73, s17, 0\n\t"
      "s_add_u32 s50, s14, s76\n\t"
      "s_addc_u32 s51, s15, 0\n\t"
      "global_load_dwordx4 v[154:157], v88, s[72:73]\n\t"
      "global_load_dword v158, v88, s[72:73] offset:16\n\t"
      "global_load_dwordx4 v[180:183], v89, s[50:51]\n\t"
      "global_load_dwordx4 v[184:187], v89, s[50:51] offset:16\n\t"
      "global_load_dwordx2 v[144:145], v89, s[50:51] offset:32\n\t"
      "s_add_u32 s86, s24, s74\n\t"
      "s_addc_u32 s87, s25, 0\n\t"
      "s_barrier\n\t"
      "s_setprio 2\n\t"
      "s_waitcnt vmcnt(5)\n\t"
      "s_mov_b64 exec, s[56:57]\n\t"
      "v_mul_u32_u24_sdwa v165, s78, v148 dst_sel:DWORD dst_unused:UNUSED_PAD src0_sel:DWORD src1_sel:BYTE_1\n\t"
      "v_lshlrev_b32_sdwa v160, 2, v148 dst_sel:DWORD dst_unused:UNUSED_PAD src0_sel:DWORD src1_sel:BYTE_0\n\t"
      "v_lshrrev_b32 v88, 11, v165\n\t"
      "v_and_b32 v148, 0xff00, v148\n\t"
      "v_lshlrev_b32 v165, 3, v88\n\t"
      "v_lshlrev_b32 v148, 9, v148\n\t"
      "v_lshlrev_b32 v175, v165, v85\n\t"
      "v_mad_i32_i24 v148, v88, s79, v148\n\t"
      "ds_add_rtn_u32 v170, v160, v175 offset:43264\n\t"
      "v_mul_u32_u24_sdwa v166, s78, v149 dst_sel:DWORD dst_unused:UNUSED_PAD src0_sel:DWORD src1_sel:BYTE_1\n\t"
      "v_lshlrev_b32_sdwa v161, 2, v149 dst_sel:DWORD dst_unused:UNUSED_PAD src0_sel:DWORD src1_sel:BYTE_0\n\t"
      "v_lshrrev_b32 v88, 11, v166\n\t"
      "v_and_b32 v149, 0xff00, v149\n\t"
      "v_lshlrev_b32 v166, 3, v88\n\t"
      "v_lshlrev_b32 v149, 9, v149\n\t"
      "v_lshlrev_b32 v176, v166, v85\n\t"
      "v_mad_i32_i24 v149, v88, s79, v149\n\t"
      "ds_add_rtn_u32 v171, v161, v176 offset:43264\n\t"
      "v_mul_u32_u24_sdwa v167, s78, v150 dst_sel:DWORD dst_unused:UNUSED_PAD src0_sel:DWORD src1_sel:BYTE_1\n\t"
      "v_lshlrev_b32_sdwa v162, 2, v150 dst_sel:DWORD dst_unused:UNUSED_PAD src0_sel:DWORD src1_sel:BYTE_0\n\t"
      "v_lshrrev_b32 v88, 11, v167\n\t"
      "v_and_b32 v150, 0xff00, v150\n\t"
      "v_lshlrev_b32 v167, 3, v88\n\t"
      "v_lshlrev_b32 v150, 9, v150\n\t"
      "v_lshlrev_b32 v177, v167, v85\n\t"
      "v_mad_i32_i24 v150, v88, s79, v150\n\t"
      "ds_add_rtn_u32 v172, v162, v177 offset:43264\n\t"
      "v_mul_u32_u24_sdwa v168, s78, v151 dst_sel:DWORD dst_unused:UNUSED_PAD src0_sel:DWORD src1_sel:BYTE_1\n\t"
      "v_lshlrev_b32_sdwa v163, 2, v151 dst_sel:DWORD dst_unused:UNUSED_PAD src0_sel:DWORD src1_sel:BYTE_0\n\t"
      "v_lshrrev_b32 v88, 11, v168\n\t"
      "v_and_b32 v151, 0xff00, v151\n\t"
      "v_lshlrev_b32 v168, 3, v88\n\t"
      "v_lshlrev_b32 v151, 9, v151\n\t"
      "v_lshlrev_b32 v178, v168, v85\n\t"
      "v_mad_i32_i24 v151, v88, s79, v151\n\t"
      "ds_add_rtn_u32 v173, v163, v178 offset:43264\n\t"
      "v_mul_u32_u24_sdwa v169, s78, v152 dst_sel:DWORD dst_unused:UNUSED_PAD src0_sel:DWORD src1_sel:BYTE_1\n\t"
      "v_lshlrev_b32_sdwa v164, 2, v152 dst_sel:DWORD dst_unused:UNUSED_PAD src0_sel:DWORD src1_sel:BYTE_0\n\t"
      "v_lshrrev_b32 v88, 11, v169\n\t"
      "v_and_b32 v152, 0xff00, v152\n\t"
      "v_lshlrev_b32 v169, 3, v88\n\t"
      "v_lshlrev_b32 v152, 9, v152\n\t"
      "v_lshlrev_b32 v179, v169, v85\n\t"
      "v_mad_i32_i24 v152, v88, s79, v152\n\t"
      "ds_add_rtn_u32 v174, v164, v179 offset:43264\n\t"
      "s_mov_b64 exec, -1\n\t"
      "global_load_dwordx4 v[108:111], v58, s[86:87] offset:0 sc1\n\t"
      "global_load_dwordx4 v[112:115], v58, s[86:87] offset:1024 sc1\n\t"
      "s_add_u32 s84, s24, s93\n\t"
      "s_addc_u32 s85, s25, 0\n\t"
      "global_load_dword v98, v94, s[84:85] offset:4 sc1\n\t"
      "s_mov_b64 exec, 1\n\t"
      "ds_add_u32 v99, v127 offset:32\n\t"
      "s_mov_b64 exec, -1\n\t"
      "v_add_u32 v88, 92416, v97\n\t"
      "v_lshlrev_b32 v91, 1, v58\n\t"
      "v_lshlrev_b32 v160, 1, v160\n\t"
      "v_lshlrev_b32 v161, 1, v161\n\t"
      "v_lshlrev_b32 v162, 1, v162\n\t"
      "v_lshlrev_b32 v163, 1, v163\n\t"
      "v_lshlrev_b32 v164, 1, v164\n\t"
      "v_add_u32 v175, -1, v175\n\t"
      "v_add_u32 v176, -1, v176\n\t"
      "v_add_u32 v177, -1, v177\n\t"
      "v_add_u32 v178, -1, v178\n\t"
      "v_add_u32 v179, -1, v179\n\t"
      "Law1_%=:\n\t"
      "ds_read_b32 v103, v99 offset:32\n\t"
      "s_waitcnt lgkmcnt(0)\n\t"
      "v_cmp_ne_u32 vcc, 4, v103\n\t"
      "s_cbranch_vccnz Law1_%=\n\t"
      "ds_read_b128 v[188:191], v58 offset:43264\n\t"
      "s_waitcnt vmcnt(3)\n\t"
      "v_mov_b64 v[128:129], v[180:181]\n\t"
      "v_bfe_u32 v170, v170, v165, 8\n\t"
      "v_add_u32 v130, v154, v148\n\t"
      "v_lshlrev_b32 v165, 1, v165\n\t"
      "v_lshl_or_b32 v131, v170, 21, v165\n\t"
      "ds_write_b128 v88, v[128:131] offset:0\n\t"
      "v_mov_b64 v[132:133], v[182:183]\n\t"
      "v_bfe_u32 v171, v171, v166, 8\n\t"
      "v_add_u32 v134, v155, v149\n\t"
      "v_lshlrev_b32 v166, 1, v166\n\t"
      "v_lshl_or_b32 v135, v171, 21, v166\n\t"
      "ds_write_b128 v88, v[132:135] offset:4096\n\t"
      "s_waitcnt lgkmcnt(2)\n\t"
      "v_sad_u8 v192, v188, 0, 0\n\t"
      "v_sad_u8 v193, v189, 0, v192\n\t"
      "v_sad_u8 v194, v190, 0, v193\n\t"
      "v_sad_u8 v195, v191, 0, v194\n\t"
      "v_mov_b32 v200, v188\n\t"
      "v_mov_b32 v202, v189\n\t"
      "v_add_u32_dpp v196, v195, v195 row_shr:1 row_mask:0xf bank_mask:0xf bound_ctrl:1\n\t"
      "v_mov_b32 v204, v190\n\t"
      "v_mov_b32 v206, v191\n\t"
      "v_add_u32_dpp v196, v196, v196 row_shr:2 row_mask:0xf bank_mask:0xf bound_ctrl:1\n\t"
      "v_mov_b64 v[136:137], v[184:185]\n\t"
      "v_bfe_u32 v172, v172, v167, 8\n\t"
      "v_add_u32 v138, v156, v150\n\t"
      "v_add_u32_dpp v196, v196, v196 row_shr:4 row_mask:0xf bank_mask:0xf bound_ctrl:1\n\t"
      "v_lshlrev_b32 v167, 1, v167\n\t"
      "v_lshl_or_b32 v139, v172, 21, v167\n\t"
      "ds_write_b128 v88, v[136:139] offset:8192\n\t"
      "v_add_u32_dpp v196, v196, v196 row_shr:8 row_mask:0xf bank_mask:0xf bound_ctrl:1\n\t"
      "v_mov_b64 v[140:141], v[186:187]\n\t"
      "v_bfe_u32 v173, v173, v168, 8\n\t"
      "v_add_u32 v142, v157, v151\n\t"
      "v_add_u32_dpp v196, v196, v196 row_bcast:15 row_mask:0xa bank_mask:0xf\n\t"
      "v_lshlrev_b32 v168, 1, v168\n\t"
      "v_lshl_or_b32 v143, v173, 21, v168\n\t"
      "ds_write_b128 v88, v[140:143] offset:12288\n\t"
      "v_add_u32_dpp v196, v196, v196 row_bcast:31 row_mask:0xc bank_mask:0xf\n\t"
      "s_mul_i32 s78, %[r], 20096\n\t"
      "s_add_u32 s78, s78, 2568192\n\t"
      "v_sub_u32 v201, v196, v195\n\t"
      "v_readlane_b32 s74, v196, 63\n\t"
      "v_add_u32 v203, v201, v192\n\t"
      "v_add_u32 v205, v201, v193\n\t"
      "v_add_u32 v207, v201, v194\n\t"
      "ds_write_b128 v91, v[200:203] offset:44288\n\t"
      "ds_write_b128 v91, v[204:207] offset:44304\n\t"
      "s_mov_b64 exec, s[56:57]\n\t"
      "ds_read_b64 v[200:201], v160 offset:44288\n\t"
      "ds_read_b64 v[202:203], v161 offset:44288\n\t"
      "ds_read_b64 v[204:205], v162 offset:44288\n\t"
      "ds_read_b64 v[206:207], v163 offset:44288\n\t"
      "ds_read_b64 v[208:209], v164 offset:44288\n\t"
      "v_bfe_u32 v174, v174, v169, 8\n\t"
      "v_add_u32 v146, v158, v152\n\t"
      "v_lshlrev_b32 v169, 1, v169\n\t"
      "v_lshl_or_b32 v147, v174, 21, v169\n\t"
      "ds_write_b128 v88, v[144:147] offset:16384\n\t"
      "s_add_u32 s50, s24, s78\n\t"
      "s_addc_u32 s51, s25, 0\n\t"
      "s_mov_b32 s93, 112896\n\t"
      "v_add_u32 v154, 0, v97\n\t"
      "v_add_u32 v155, 4096, v97\n\t"
      "v_add_u32 v156, 8192, v97\n\t"
      "v_add_u32 v157, 12288, v97\n\t"
      "v_add_u32 v158, 16384, v97\n\t"
      "s_waitcnt lgkmcnt(5)\n\t"
      "v_and_b32 v200, v200, v175\n\t"
      "v_add_u32 v201, v201, v170\n\t"
      "v_sad_u8 v200, v200, 0, v201\n\t"
      "v_min_u32 v200, 1249, v200\n\t"
      "v_lshl_add_u32 v200, v200, 1, s93\n\t"
      "ds_write_b16 v200, v154\n\t"
      "s_waitcnt lgkmcnt(5)\n\t"
      "v_and_b32 v202, v202, v176\n\t"
      "v_add_u32 v203, v203, v171\n\t"
      "v_sad_u8 v202, v202, 0, v203\n\t"
      "v_min_u32 v202, 1249, v202\n\t"
      "v_lshl_add_u32 v202, v202, 1, s93\n\t"
      "ds_write_b16 v202, v155\n\t"
      "s_waitcnt lgkmcnt(5)\n\t"
      "v_and_b32 v204, v204, v177\n\t"
      "v_add_u32 v205, v205, v172\n\t"
      "v_sad_u8 v204, v204, 0, v205\n\t"
      "v_min_u32 v204, 1249, v204\n\t"
      "v_lshl_add_u32 v204, v204, 1, s93\n\t"
      "ds_write_b16 v204, v156\n\t"
      "s_waitcnt lgkmcnt(5)\n\t"
      "v_and_b32 v206, v206, v178\n\t"
      "v_add_u32 v207, v207, v173\n\t"
      "v_sad_u8 v206, v206, 0, v207\n\t"
      "v_min_u32 v206, 1249, v206\n\t"
      "v_lshl_add_u32 v206, v206, 1, s93\n\t"
      "ds_write_b16 v206, v157\n\t"
      "s_waitcnt lgkmcnt(5)\n\t"
      "v_and_b32 v208, v208, v179\n\t"
      "v_add_u32 v209, v209, v174\n\t"
      "v_sad_u8 v208, v208, 0, v209\n\t"
      "v_min_u32 v208, 1249, v208\n\t"
      "v_lshl_add_u32 v208, v208, 1, s93\n\t"
      "ds_write_b16 v208, v158\n\t"
      "s_mov_b64 exec, 1\n\t"
      "ds_add_u32 v99, v127 offset:36\n\t"
      "s_mov_b64 exec, -1\n\t"
      "ds_read_b128 v[240:243], v58 offset:44288\n\t"
      "ds_read_b128 v[244:247], v58 offset:45312\n\t"
      "v_lshrrev_b32 v88, 3, v97\n\t"
      "v_add_u32 v88, 112896, v88\n\t"
      "v_add_u32 v89, 0x2000, v97\n\t"
      "v_add_u32 v90, 0x3000, v97\n\t"
      "s_mov_b32 s93, 92416\n\t"
      "Law2_%=:\n\t"
      "ds_read_b32 v103, v99 offset:36\n\t"
      "s_waitcnt lgkmcnt(0)\n\t"
      "v_cmp_ne_u32 vcc, 4, v103\n\t"
      "s_cbranch_vccnz Law2_%=\n\t"
      "ds_read_u16 v154, v88 offset:0\n\t"
      "ds_read_u16 v155, v88 offset:512\n\t"
      "ds_read_u16 v156, v88 offset:1024\n\t"
      "ds_read_u16 v157, v88 offset:1536\n\t"
      "ds_read_u16 v158, v88 offset:2048\n\t"
      "s_waitcnt lgkmcnt(4)\n\t"
      "v_and_b32 v154, 0x7ff0, v154\n\t"
      "v_add_u32 v154, s93, v154\n\t"
      "ds_read_b128 v[128:131], v154\n\t"
      "s_waitcnt lgkmcnt(4)\n\t"
      "v_and_b32 v155, 0x7ff0, v155\n\t"
      "v_add_u32 v155, s93, v155\n\t"
      "ds_read_b128 v[132:135], v155\n\t"
      "s_waitcnt lgkmcnt(4)\n\t"
      "v_and_b32 v156, 0x7ff0, v156\n\t"
      "v_add_u32 v156, s93, v156\n\t"
      "ds_read_b128 v[136:139], v156\n\t"
      "s_waitcnt lgkmcnt(4)\n\t"
      "v_and_b32 v157, 0x7ff0, v157\n\t"
      "v_add_u32 v157, s93, v157\n\t"
      "ds_read_b128 v[140:143], v157\n\t"
      "s_waitcnt lgkmcnt(4)\n\t"
      "v_and_b32 v158, 0x7ff0, v158\n\t"
      "v_add_u32 v158, s93, v158\n\t"
      "ds_read_b128 v[144:147], v158\n\t"
      "s_waitcnt lgkmcnt(4)\n\t"
      "global_store_dwordx4 v97, v[128:131], s[50:51] offset:-4096 sc1\n\t"
      "s_waitcnt lgkmcnt(3)\n\t"
      "global_store_dwordx4 v97, v[132:135], s[50:51] offset:0 sc1\n\t"
      "s_waitcnt lgkmcnt(2)\n\t"
      "global_store_dwordx4 v89, v[136:139], s[50:51] offset:-4096 sc1\n\t"
      "s_waitcnt lgkmcnt(1)\n\t"
      "global_store_dwordx4 v89, v[140:143], s[50:51] offset:0 sc1\n\t"
      "s_waitcnt lgkmcnt(0)\n\t"
      "s_mov_b64 exec, s[52:53]\n\t"
      "global_store_dwordx4 v90, v[144:147], s[50:51] offset:0 sc1\n\t"
      "s_mov_b64 exec, 1\n\t"
      "ds_add_u32 v99, v127 offset:40\n\t"
      "s_mov_b64 exec, -1\n\t"
      "s_setprio 0\n\t"
      "s_cmp_lg_u32 s74, 1250\n\t"
      "s_cselect_b32 s74, -1, 0\n\t"
      "s_mov_b32 s99, 0x10000\n\t"
      "s_waitcnt vmcnt(5)\n\t"
      "v_and_b32 v109, 0xffff0000, v109\n\t"
      "v_or_b32 v240, s74, v240\n\t"
      "v_add3_u32 v241, v241, v109, s99\n\t"
      "v_and_b32 v111, 0xffff0000, v111\n\t"
      "v_or_b32 v242, s74, v242\n\t"
      "v_add3_u32 v243, v243, v111, s99\n\t"
      "v_and_b32 v113, 0xffff0000, v113\n\t"
      "v_or_b32 v244, s74, v244\n\t"
      "v_add3_u32 v245, v245, v113, s99\n\t"
      "v_and_b32 v115, 0xffff0000, v115\n\t"
      "v_or_b32 v246, s74, v246\n\t"
      "v_add3_u32 v247, v247, v115, s99\n\t"
      "s_branch Lfj_%=\n\t"
      "Lfp_%=:\n\t"
      "s_mov_b32 s77, s48\n\t"
      "s_and_b32 s78, %[b], 7\n\t"
      "s_lshl_b32 s78, s78, 5\n\t"
      "s_lshr_b32 s79, %[b], 3\n\t"
      "s_add_u32 s78, s78, s79\n\t"
      "s_lshl_b32 s92, s78, 8\n\t"
      "s_lshl_b32 s93, s78, 3\n\t"
      "s_add_u32 s93, s93, 5136384\n\t"
      "v_lshrrev_b32 v91, 1, v0\n\t"
      "v_lshlrev_b32 v94, 11, v91\n\t"
      "v_mul_u32_u24 v84, 20096, v91\n\t"
      "v_lshlrev_b32 v97, 4, v0\n\t"
      "v_and_b32 v82, 1, v0\n\t"
      "v_and_b32 v86, 31, v0\n\t"
      "v_bfe_u32 v87, v0, 5, 1\n\t"
      "v_lshrrev_b32 v93, 4, v0\n\t"
      "v_bfe_u32 v92, v0, 4, 2\n\t"
      "v_and_b32 v58, 63, v0\n\t"
      "v_lshlrev_b32 v58, 4, v58\n\t"
      "s_mov_b32 s98, 0\n\t"
      "s_mov_b32 s80, 0xbfb8aa3b\n\t"
      "s_mov_b32 s81, 0xbfb8aa3b\n\t"
      "v_mov_b32 v99, 32768\n\t"
      "v_mov_b32 v127, 1\n\t"
      "v_and_b32 v91, 15, v0\n\t"
      "v_lshlrev_b32 v90, 2, v92\n\t"
      "v_xor_b32 v90, v91, v90\n\t"
      "v_lshlrev_b32 v90, 4, v90\n\t"
      "v_lshl_or_b32 v252, v92, 8, v90\n\t"
      "v_xor_b32 v253, 16, v252\n\t"
      "v_xor_b32 v254, 32, v252\n\t"
      "v_xor_b32 v255, 48, v252\n\t"
      "v_xor_b32 v90, v93, v91\n\t"
      "v_lshlrev_b32 v90, 4, v90\n\t"
      "v_lshl_or_b32 v104, v93, 8, v90\n\t"
      "s_sub_u32 s74, %[r], 128\n\t"
      "s_mul_i32 s54, s74, 20480\n\t"
      "s_lshl_b32 s74, s74, 7\n\t"
      "s_add_u32 s74, s74, 5398528\n\t"
      "s_lshl_b32 s78, s48, 10\n\t"
      "s_add_u32 m0, s78, 125184\n\t"
      "v_mov_b32 v126, 0\n\t"
      "s_waitcnt lgkmcnt(0)\n\t"
      "s_add_u32 s86, s24, s74\n\t"
      "s_addc_u32 s87, s25, 0\n\t"
      "s_cmp_ge_u32 %[r], 253\n\t"
      "s_cbranch_scc1 Lfi_%=\n\t"
      "s_add_u32 s50, s12, s54\n\t"
      "s_addc_u32 s51, s13, 0\n\t"
      "global_load_lds_dwordx4 v104, s[50:51]\n\t"
      "s_add_u32 m0, s78, 129280\n\t"
      "v_add_u32 v88, 4096, v104\n\t"
      "global_load_lds_dwordx4 v88, s[50:51]\n\t"
      "s_add_u32 m0, s78, 133376\n\t"
      "v_add_u32 v88, 8192, v104\n\t"
      "global_load_lds_dwordx4 v88, s[50:51]\n\t"
      "s_add_u32 m0, s78, 137472\n\t"
      "v_add_u32 v88, 12288, v104\n\t"
      "global_load_lds_dwordx4 v88, s[50:51]\n\t"
      "s_add_u32 m0, s78, 141568\n\t"
      "v_add_u32 v88, 16384, v104\n\t"
      "global_load_lds_dwordx4 v88, s[50:51]\n\t"
      "s_lshl_b32 s79, s77, 13\n\t"
      "s_add_u32 s75, s79, 0\n\t"
      "s_add_u32 s70, s20, s75\n\t"
      "s_addc_u32 s71, s21, 0\n\t"
      "s_add_u32 s72, s70, 4096\n\t"
      "s_addc_u32 s73, s71, 0\n\t"
      "s_add_u32 m0, s79, 92416\n\t"
      "s_nop 0\n\t"
      "global_load_lds_dwordx4 v252, s[70:71] offset:0\n\t"
      "global_load_lds_dwordx4 v253, s[70:71] offset:1024\n\t"
      "global_load_lds_dwordx4 v254, s[70:71] offset:2048\n\t"
      "global_load_lds_dwordx4 v255, s[70:71] offset:3072\n\t"
      "s_add_u32 m0, s79, 96512\n\t"
      "s_nop 0\n\t"
      "global_load_lds_dwordx4 v252, s[72:73] offset:0\n\t"
      "global_load_lds_dwordx4 v253, s[72:73] offset:1024\n\t"
      "global_load_lds_dwordx4 v254, s[72:73] offset:2048\n\t"
      "global_load_lds_dwordx4 v255, s[72:73] offset:3072\n\t"
      "s_barrier\n\t"
      "s_lshl_b32 s76, s77, 2\n\t"
      "v_bfe_u32 v90, v0, 2, 2\n\t"
      "v_lshl_or_b32 v89, v87, 3, v90\n\t"
      "v_lshlrev_b32 v90, 2, v90\n\t"
      "v_lshl_or_b32 v90, v87, 1, v90\n\t"
      "v_bfe_u32 v91, v0, 1, 1\n\t"
      "v_and_b32 v88, 16, v0\n\t"
      "v_lshrrev_b32 v88, 3, v88\n\t"
      "v_or3_b32 v91, v91, v88, s76\n\t"
      "v_xor_b32 v90, v90, v91\n\t"
      "v_lshlrev_b32 v90, 4, v90\n\t"
      "v_lshl_or_b32 v90, v89, 8, v90\n\t"
      "v_and_b32 v88, 1, v0\n\t"
      "v_lshl_or_b32 v90, v88, 3, v90\n\t"
      "v_add_u32 v109, 92416, v90\n\t"
      "v_xor_b32 v90, 16, v90\n\t"
      "v_add_u32 v110, 93440, v90\n\t"
      "v_and_b32 v88, 15, v86\n\t"
      "v_xor_b32 v89, v88, v87\n\t"
      "v_lshlrev_b32 v89, 4, v89\n\t"
      "v_lshl_or_b32 v89, v86, 8, v89\n\t"
      "v_add_u32 v224, 125184, v89\n\t"
      "v_xor_b32 v225, 32, v224\n\t"
      "v_xor_b32 v226, 64, v224\n\t"
      "v_xor_b32 v227, 96, v224\n\t"
      "v_xor_b32 v228, 128, v224\n\t"
      "v_xor_b32 v229, 160, v224\n\t"
      "v_xor_b32 v230, 192, v224\n\t"
      "v_xor_b32 v231, 224, v224\n\t"
      "v_mul_u32_u24 v88, 3, v87\n\t"
      "v_xor_b32 v88, s76, v88\n\t"
      "v_lshlrev_b32 v88, 4, v88\n\t"
      "v_xor_b32 v88, v88, v89\n\t"
      "v_add_u32 v120, 43264, v88\n\t"
      "v_xor_b32 v121, 16, v120\n\t"
      "v_add_u32 v122, 43264, v104\n\t"
      "v_add_u32 v123, 149760, v104\n\t"
      "v_add_u32 v232, 149760, v88\n\t"
      "v_xor_b32 v233, 16, v232\n\t"
      "v_add_u32 v89, 0x2000, v97\n\t"
      "v_add_u32 v90, 0x3000, v97\n\t"
      "s_add_u32 s78, s54, 8192\n\t"
      "s_add_u32 s50, s24, s78\n\t"
      "s_addc_u32 s51, s25, 0\n\t"
      "s_setprio 2\n\t"
      "s_waitcnt vmcnt(0)\n\t"
      "global_load_dword v125, v126, s[86:87] sc1\n\t"
      "s_add_u32 s84, s24, s93\n\t"
      "s_addc_u32 s85, s25, 0\n\t"
      "global_load_dword v98, v94, s[84:85] offset:4 sc1\n\t"
      "s_mov_b64 exec, 1\n\t"
      "ds_add_u32 v99, v127 offset:32\n\t"
      "s_mov_b64 exec, -1\n\t"
      "Law3_%=:\n\t"
      "ds_read_b32 v103, v99 offset:32\n\t"
      "s_waitcnt lgkmcnt(0)\n\t"
      "v_cmp_ne_u32 vcc, 4, v103\n\t"
      "s_cbranch_vccnz Law3_%=\n\t"
      "ds_read_b64_tr_b16 v[152:153], v109 offset:0\n\t"
      "ds_read_b64_tr_b16 v[154:155], v110 offset:0\n\t"
      "ds_read_b128 v[128:131], v224 offset:0\n\t"
      "ds_read_b64_tr_b16 v[156:157], v109 offset:4096\n\t"
      "ds_read_b64_tr_b16 v[158:159], v110 offset:4096\n\t"
      "ds_read_b128 v[132:135], v225 offset:0\n\t"
      "ds_read_b64_tr_b16 v[160:161], v109 offset:8192\n\t"
      "ds_read_b64_tr_b16 v[162:163], v110 offset:8192\n\t"
      "ds_read_b128 v[136:139], v226 offset:0\n\t"
      "ds_read_b64_tr_b16 v[164:165], v109 offset:12288\n\t"
      "ds_read_b64_tr_b16 v[166:167], v110 offset:12288\n\t"
      "ds_read_b128 v[140:143], v227 offset:0\n\t"
      "s_waitcnt lgkmcnt(9)\n\t"
      "v_mfma_f32_32x32x16_bf16 v[184:199], v[152:155], v[128:131], 0\n\t"
      "ds_read_b64_tr_b16 v[168:169], v109 offset:16384\n\t"
      "ds_read_b64_tr_b16 v[170:171], v110 offset:16384\n\t"
      "ds_read_b128 v[144:147], v228 offset:0\n\t"
      "s_waitcnt lgkmcnt(9)\n\t"
      "v_mfma_f32_32x32x16_bf16 v[184:199], v[156:159], v[132:135], v[184:199]\n\t"
      "ds_read_b64_tr_b16 v[172:173], v109 offset:20480\n\t"
      "ds_read_b64_tr_b16 v[174:175], v110 offset:20480\n\t"
      "ds_read_b128 v[148:151], v229 offset:0\n\t"
      "s_waitcnt lgkmcnt(9)\n\t"
      "v_mfma_f32_32x32x16_bf16 v[184:199], v[160:163], v[136:139], v[184:199]\n\t"
      "ds_read_b64_tr_b16 v[176:177], v109 offset:24576\n\t"
      "ds_read_b64_tr_b16 v[178:179], v110 offset:24576\n\t"
      "ds_read_b128 v[128:131], v230 offset:0\n\t"
      "s_waitcnt lgkmcnt(9)\n\t"
      "v_mfma_f32_32x32x16_bf16 v[184:199], v[164:167], v[140:143], v[184:199]\n\t"
      "ds_read_b64_tr_b16 v[180:181], v109 offset:28672\n\t"
      "ds_read_b64_tr_b16 v[182:183], v110 offset:28672\n\t"
      "ds_read_b128 v[132:135], v231 offset:0\n\t"
      "s_waitcnt lgkmcnt(9)\n\t"
      "v_mfma_f32_32x32x16_bf16 v[184:199], v[168:171], v[144:147], v[184:199]\n\t"
      "ds_read_b128 v[136:139], v224 offset:8192\n\t"
      "s_waitcnt lgkmcnt(7)\n\t"
      "v_mfma_f32_32x32x16_bf16 v[184:199], v[172:175], v[148:151], v[184:199]\n\t"
      "ds_read_b128 v[140:143], v225 offset:8192\n\t"
      "s_waitcnt lgkmcnt(5)\n\t"
      "v_mfma_f32_32x32x16_bf16 v[184:199], v[176:179], v[128:131], v[184:199]\n\t"
      "ds_read_b128 v[144:147], v226 offset:8192\n\t"
      "s_waitcnt lgkmcnt(3)\n\t"
      "v_mfma_f32_32x32x16_bf16 v[184:199], v[180:183], v[132:135], v[184:199]\n\t"
      "ds_read_b128 v[148:151], v227 offset:8192\n\t"
      "s_waitcnt lgkmcnt(3)\n\t"
      "v_mfma_f32_32x32x16_bf16 v[200:215], v[152:155], v[136:139], 0\n\t"
      "ds_read_b128 v[128:131], v228 offset:8192\n\t"
      "s_waitcnt lgkmcnt(3)\n\t"
      "v_mfma_f32_32x32x16_bf16 v[200:215], v[156:159], v[140:143], v[200:215]\n\t"
      "ds_read_b128 v[132:135], v229 offset:8192\n\t"
      "s_waitcnt lgkmcnt(3)\n\t"
      "v_mfma_f32_32x32x16_bf16 v[200:215], v[160:163], v[144:147], v[200:215]\n\t"
      "ds_read_b128 v[136:139], v230 offset:8192\n\t"
      "s_waitcnt lgkmcnt(3)\n\t"
      "v_mfma_f32_32x32x16_bf16 v[200:215], v[164:167], v[148:151], v[200:215]\n\t"
      "ds_read_b128 v[140:143], v231 offset:8192\n\t"
      "v_mul_f32 v184, s80, v184\n\t"
      "v_mul_f32 v185, s80, v185\n\t"
      "v_cvt_pk_bf16_f32 v184, v184, v185\n\t"
      "v_mul_f32 v186, s80, v186\n\t"
      "v_mul_f32 v187, s80, v187\n\t"
      "v_cvt_pk_bf16_f32 v185, v186, v187\n\t"
      "s_waitcnt lgkmcnt(3)\n\t"
      "v_mfma_f32_32x32x16_bf16 v[200:215], v[168:171], v[128:131], v[200:215]\n\t"
      "ds_read_b128 v[144:147], v224 offset:16384\n\t"
      "v_mul_f32 v188, s80, v188\n\t"
      "v_mul_f32 v189, s80, v189\n\t"
      "v_cvt_pk_bf16_f32 v186, v188, v189\n\t"
      "v_mul_f32 v190, s80, v190\n\t"
      "v_mul_f32 v191, s80, v191\n\t"
      "v_cvt_pk_bf16_f32 v187, v190, v191\n\t"
      "s_waitcnt lgkmcnt(3)\n\t"
      "v_mfma_f32_32x32x16_bf16 v[200:215], v[172:175], v[132:135], v[200:215]\n\t"
      "ds_read_b128 v[148:151], v225 offset:16384\n\t"
      "v_mul_f32 v192, s80, v192\n\t"
      "v_mul_f32 v193, s80, v193\n\t"
      "v_cvt_pk_bf16_f32 v188, v192, v193\n\t"
      "v_mul_f32 v194, s80, v194\n\t"
      "v_mul_f32 v195, s80, v195\n\t"
      "v_cvt_pk_bf16_f32 v189, v194, v195\n\t"
      "s_waitcnt lgkmcnt(3)\n\t"
      "v_mfma_f32_32x32x16_bf16 v[200:215], v[176:179], v[136:139], v[200:215]\n\t"
      "ds_read_b128 v[128:131], v226 offset:16384\n\t"
      "v_mul_f32 v196, s80, v196\n\t"
      "v_mul_f32 v197, s80, v197\n\t"
      "v_cvt_pk_bf16_f32 v190, v196, v197\n\t"
      "v_mul_f32 v198, s80, v198\n\t"
      "v_mul_f32 v199, s80, v199\n\t"
      "v_cvt_pk_bf16_f32 v191, v198, v199\n\t"
      "s_waitcnt lgkmcnt(3)\n\t"
      "v_mfma_f32_32x32x16_bf16 v[200:215], v[180:183], v[140:143], v[200:215]\n\t"
      "ds_read_b128 v[132:135], v227 offset:16384\n\t"
      "ds_write_b128 v120, v[184:187] offset:0\n\t"
      "ds_write_b128 v121, v[188:191] offset:0\n\t"
      "s_waitcnt lgkmcnt(5)\n\t"
      "v_mfma_f32_32x32x16_bf16 v[2:17], v[152:155], v[144:147], 0\n\t"
      "ds_read_b128 v[136:139], v228 offset:16384\n\t"
      "s_waitcnt lgkmcnt(5)\n\t"
      "v_mfma_f32_32x32x16_bf16 v[2:17], v[156:159], v[148:151], v[2:17]\n\t"
      "ds_read_b128 v[140:143], v229 offset:16384\n\t"
      "s_waitcnt lgkmcnt(5)\n\t"
      "v_mfma_f32_32x32x16_bf16 v[2:17], v[160:163], v[128:131], v[2:17]\n\t"
      "ds_read_b128 v[144:147], v230 offset:16384\n\t"
      "s_waitcnt lgkmcnt(5)\n\t"
      "v_mfma_f32_32x32x16_bf16 v[2:17], v[164:167], v[132:135], v[2:17]\n\t"
      "ds_read_b128 v[148:151], v231 offset:16384\n\t"
      "v_mul_f32 v200, s80, v200\n\t"
      "v_mul_f32 v201, s80, v201\n\t"
      "v_cvt_pk_bf16_f32 v200, v200, v201\n\t"
      "v_mul_f32 v202, s80, v202\n\t"
      "v_mul_f32 v203, s80, v203\n\t"
      "v_cvt_pk_bf16_f32 v201, v202, v203\n\t"
      "s_waitcnt lgkmcnt(3)\n\t"
      "v_mfma_f32_32x32x16_bf16 v[2:17], v[168:171], v[136:139], v[2:17]\n\t"
      "v_mul_f32 v204, s80, v204\n\t"
      "v_mul_f32 v205, s80, v205\n\t"
      "v_cvt_pk_bf16_f32 v202, v204, v205\n\t"
      "v_mul_f32 v206, s80, v206\n\t"
      "v_mul_f32 v207, s80, v207\n\t"
      "v_cvt_pk_bf16_f32 v203, v206, v207\n\t"
      "s_waitcnt lgkmcnt(2)\n\t"
      "v_mfma_f32_32x32x16_bf16 v[2:17], v[172:175], v[140:143], v[2:17]\n\t"
      "v_mul_f32 v208, s80, v208\n\t"
      "v_mul_f32 v209, s80, v209\n\t"
      "v_cvt_pk_bf16_f32 v204, v208, v209\n\t"
      "v_mul_f32 v210, s80, v210\n\t"
      "v_mul_f32 v211, s80, v211\n\t"
      "v_cvt_pk_bf16_f32 v205, v210, v211\n\t"
      "s_waitcnt lgkmcnt(1)\n\t"
      "v_mfma_f32_32x32x16_bf16 v[2:17], v[176:179], v[144:147], v[2:17]\n\t"
      "v_mul_f32 v212, s80, v212\n\t"
      "v_mul_f32 v213, s80, v213\n\t"
      "v_cvt_pk_bf16_f32 v206, v212, v213\n\t"
      "v_mul_f32 v214, s80, v214\n\t"
      "v_mul_f32 v215, s80, v215\n\t"
      "v_cvt_pk_bf16_f32 v207, v214, v215\n\t"
      "s_waitcnt lgkmcnt(0)\n\t"
      "v_mfma_f32_32x32x16_bf16 v[2:17], v[180:183], v[148:151], v[2:17]\n\t"
      "ds_write_b128 v120, v[200:203] offset:8192\n\t"
      "ds_write_b128 v121, v[204:207] offset:8192\n\t"
      "s_nop 9\n\t"
      "v_pk_mul_f32 v[2:3], v[2:3], s[80:81]\n\t"
      "v_pk_mul_f32 v[4:5], v[4:5], s[80:81]\n\t"
      "v_pk_mul_f32 v[6:7], v[6:7], s[80:81]\n\t"
      "v_pk_mul_f32 v[8:9], v[8:9], s[80:81]\n\t"
      "v_pk_mul_f32 v[10:11], v[10:11], s[80:81]\n\t"
      "v_pk_mul_f32 v[12:13], v[12:13], s[80:81]\n\t"
      "v_pk_mul_f32 v[14:15], v[14:15], s[80:81]\n\t"
      "v_pk_mul_f32 v[16:17], v[16:17], s[80:81]\n\t"
      "v_cvt_pk_bf16_f32 v128, v2, v3\n\t"
      "v_cvt_pk_bf16_f32 v129, v4, v5\n\t"
      "v_cvt_pk_bf16_f32 v130, v6, v7\n\t"
      "v_cvt_pk_bf16_f32 v131, v8, v9\n\t"
      "v_cvt_pk_bf16_f32 v132, v10, v11\n\t"
      "v_cvt_pk_bf16_f32 v133, v12, v13\n\t"
      "v_cvt_pk_bf16_f32 v134, v14, v15\n\t"
      "v_cvt_pk_bf16_f32 v135, v16, v17\n\t"
      "ds_write_b128 v232, v[128:131]\n\t"
      "ds_write_b128 v233, v[132:135]\n\t"
      "s_mov_b64 exec, 1\n\t"
      "ds_add_u32 v99, v127 offset:36\n\t"
      "s_mov_b64 exec, -1\n\t"
      "Law4_%=:\n\t"
      "ds_read_b32 v103, v99 offset:36\n\t"
      "s_waitcnt lgkmcnt(0)\n\t"
      "v_cmp_ne_u32 vcc, 4, v103\n\t"
      "s_cbranch_vccnz Law4_%=\n\t"
      "ds_read_b128 v[128:131], v122 offset:0\n\t"
      "ds_read_b128 v[132:135], v122 offset:4096\n\t"
      "ds_read_b128 v[136:139], v122 offset:8192\n\t"
      "ds_read_b128 v[140:143], v122 offset:12288\n\t"
      "ds_read_b128 v[144:147], v123\n\t"
      "s_waitcnt lgkmcnt(4)\n\t"
      "global_store_dwordx4 v97, v[128:131], s[50:51] offset:-4096 sc1\n\t"
      "s_waitcnt lgkmcnt(3)\n\t"
      "global_store_dwordx4 v97, v[132:135], s[50:51] offset:0 sc1\n\t"
      "s_waitcnt lgkmcnt(2)\n\t"
      "global_store_dwordx4 v89, v[136:139], s[50:51] offset:-4096 sc1\n\t"
      "s_waitcnt lgkmcnt(1)\n\t"
      "global_store_dwordx4 v89, v[140:143], s[50:51] offset:0 sc1\n\t"
      "s_waitcnt lgkmcnt(0)\n\t"
      "global_store_dwordx4 v90, v[144:147], s[50:51] offset:0 sc1\n\t"
      "s_mov_b64 exec, 1\n\t"
      "ds_add_u32 v99, v127 offset:40\n\t"
      "s_mov_b64 exec, -1\n\t"
      "s_setprio 0\n\t"
      "s_branch Lfj_%=\n\t"
      "Lfi_%=:\n\t"
      "global_load_dword v125, v126, s[86:87] sc1\n\t"
      "s_add_u32 s84, s24, s93\n\t"
      "s_addc_u32 s85, s25, 0\n\t"
      "global_load_dword v98, v94, s[84:85] offset:4 sc1\n\t"
      "s_barrier\n\t"
      "s_mov_b64 exec, 1\n\t"
      "ds_add_u32 v99, v127 offset:32\n\t"
      "s_mov_b64 exec, -1\n\t"
      "s_mov_b64 exec, 1\n\t"
      "ds_add_u32 v99, v127 offset:40\n\t"
      "s_mov_b64 exec, -1\n\t"
      "Lfj_%=:\n\t"
      "v_mov_b32 v85, 0x3f80\n\t"
      "v_and_b32 v96, 7, v0\n\t"
      "v_xor_b32 v89, v92, v96\n\t"
      "s_and_b32 s95, s48, 1\n\t"
      "s_lshl_b32 s95, s95, 7\n\t"
      "v_lshl_or_b32 v95, v89, 4, s95\n\t"
      "v_bfe_u32 v88, v0, 3, 3\n\t"
      "v_lshl_or_b32 v57, v88, 5, 20\n\t"
      "s_lshl_b32 s78, s48, 13\n\t"
      "s_add_u32 s42, s78, 92416\n\t"
      "v_and_b32 v88, 1, v86\n\t"
      "v_lshlrev_b32 v88, 4, v88\n\t"
      "v_lshlrev_b32 v88, v88, v85\n\t"
      "v_bfe_u32 v89, v86, 1, 1\n\t"
      "v_and_b32 v90, 4, v86\n\t"
      "v_bfe_u32 v91, v86, 3, 1\n\t"
      "v_lshl_or_b32 v89, v91, 1, v89\n\t"
      "v_lshrrev_b32 v91, 4, v86\n\t"
      "v_xor_b32 v91, v91, v87\n\t"
      "v_lshlrev_b32 v91, 3, v91\n\t"
      "v_or3_b32 v89, v89, v90, v91\n\t"
      "v_cmp_eq_u32 s[62:63], 0, v89\n\t"
      "v_cmp_eq_u32 s[64:65], 1, v89\n\t"
      "v_cmp_eq_u32 s[66:67], 2, v89\n\t"
      "v_cmp_eq_u32 s[68:69], 3, v89\n\t"
      "v_cndmask_b32 v42, 0, v88, s[62:63]\n\t"
      "v_cndmask_b32 v43, 0, v88, s[64:65]\n\t"
      "v_cndmask_b32 v44, 0, v88, s[66:67]\n\t"
      "v_cndmask_b32 v45, 0, v88, s[68:69]\n\t"
      "v_cmp_eq_u32 s[62:63], 4, v89\n\t"
      "v_cmp_eq_u32 s[64:65], 5, v89\n\t"
      "v_cmp_eq_u32 s[66:67], 6, v89\n\t"
      "v_cmp_eq_u32 s[68:69], 7, v89\n\t"
      "v_cndmask_b32 v46, 0, v88, s[62:63]\n\t"
      "v_cndmask_b32 v47, 0, v88, s[64:65]\n\t"
      "v_cndmask_b32 v48, 0, v88, s[66:67]\n\t"
      "v_cndmask_b32 v49, 0, v88, s[68:69]\n\t"
      "v_bfe_u32 v88, v0, 1, 3\n\t"
      "v_xor_b32 v88, v88, v87\n\t"
      "v_lshlrev_b32 v88, 4, v88\n\t"
      "v_lshl_add_u32 v90, v86, 7, s42\n\t"
      "v_add_u32 v50, v88, v90\n\t"
      "v_xor_b32 v51, 32, v50\n\t"
      "v_xor_b32 v52, 64, v50\n\t"
      "v_xor_b32 v53, 0x60, v50\n\t"
      "v_lshlrev_b32 v88, 4, v87\n\t"
      "v_lshl_add_u32 v54, v86, 5, v88\n\t"
      "v_and_b32 v88, 15, v0\n\t"
      "v_lshlrev_b32 v90, 3, v88\n\t"
      "v_lshl_or_b32 v55, v87, 7, v90\n\t"
      "v_lshlrev_b32 v91, 4, v88\n\t"
      "v_lshl_or_b32 v67, v93, 16, v91\n\t"
      "v_mul_u32_u24 v66, 272, v93\n\t"
      "v_add_u32 v91, 72704, v91\n\t"
      "v_add_u32 v66, v66, v91\n\t"
      "v_xor_b32 v91, v92, v88\n\t"
      "v_lshlrev_b32 v91, 4, v91\n\t"
      "v_lshl_or_b32 v60, v88, 8, v91\n\t"
      "v_xor_b32 v61, 64, v60\n\t"
      "v_xor_b32 v62, 0x80, v60\n\t"
      "v_xor_b32 v63, 0xc0, v60\n\t"
      "v_mul_u32_u24 v90, 272, v88\n\t"
      "v_add_u32 v90, 59648, v90\n\t"
      "v_lshl_add_u32 v64, v92, 4, v90\n\t"
      "v_lshl_add_u32 v65, v92, 3, v90\n\t"
      "s_lshl_b32 s79, s48, 5\n\t"
      "v_add_u32 v65, s79, v65\n\t"
      "v_mul_u32_u24 v88, 272, v86\n\t"
      "v_lshl_add_u32 v88, v87, 3, v88\n\t"
      "s_add_u32 s78, s95, 59648\n\t"
      "v_add_u32 v59, s78, v88\n\t"
      "v_cmp_gt_u32 s[96:97], 10, v86\n\t"
      "v_bfe_u32 v88, v0, 2, 2\n\t"
      "v_lshl_or_b32 v89, v92, 3, v88\n\t"
      "v_lshlrev_b32 v88, 2, v88\n\t"
      "v_and_b32 v90, 1, v92\n\t"
      "v_lshl_or_b32 v88, v90, 1, v88\n\t"
      "v_bfe_u32 v90, v0, 1, 1\n\t"
      "s_lshl_b32 s78, s48, 1\n\t"
      "v_or_b32 v90, s78, v90\n\t"
      "v_xor_b32 v88, v88, v90\n\t"
      "v_lshlrev_b32 v88, 4, v88\n\t"
      "v_lshl_or_b32 v88, v89, 8, v88\n\t"
      "v_and_b32 v90, 1, v0\n\t"
      "v_lshl_or_b32 v88, v90, 3, v88\n\t"
      "v_add_u32 v236, 59648, v88\n\t"
      "v_xor_b32 v88, 16, v88\n\t"
      "v_add_u32 v237, 60672, v88\n\t"
      "v_bfe_u32 v88, v0, 1, 5\n\t"
      "v_lshlrev_b32 v90, 4, v90\n\t"
      "v_lshl_or_b32 v238, v88, 8, v90\n\t"
      "v_and_b32 v88, 3, v0\n\t"
      "v_lshlrev_b32 v88, 3, v88\n\t"
      "v_lshl_or_b32 v88, v89, 5, v88\n\t"
      "v_add_u32 v239, s42, v88\n\t"
      "s_lshl_b32 s78, s48, 5\n\t"
      "s_add_u32 s78, s78, 32768\n\t"
      "s_add_u32 s40, s22, s78\n\t"
      "s_addc_u32 s41, s23, 0\n\t"
      "s_add_u32 s36, s24, 1024\n\t"
      "s_addc_u32 s37, s25, 0\n\t"
      "s_or_b32 s37, s37, 0x1000000\n\t"
      "s_mov_b32 s38, 10000\n\t"
      "s_mov_b32 s39, 0x20000\n\t"
      "s_mov_b32 s88, s26\n\t"
      "s_mov_b32 s89, s27\n\t"
      "s_mov_b32 s90, 2560000\n\t"
      "s_mov_b32 s91, 0x20000\n\t"
      "v_add_u32 v213, 0xc00, v95\n\t"
      "v_xor_b32 v219, 64, v95\n\t"
      "v_add_u32 v217, 0x400, v95\n\t"
      "v_add_u32 v215, 0x800, v219\n\t"
      "v_mov_b64 v[204:205], 0\n\t"
      "v_mov_b64 v[206:207], 0\n\t"
      "v_mov_b64 v[208:209], 0\n\t"
      "v_mov_b64 v[210:211], 0\n\t"
      "s_nop 0\n\t"
      "v_mfma_f32_32x32x16_bf16 v[2:17], v[204:207], v[204:207], 0\n\t"
      "v_mfma_f32_32x32x16_bf16 v[18:33], v[204:207], v[204:207], 0\n\t"
      "s_mov_b32 s74, 0x0c010c00\n\t"
      "s_mov_b32 s75, 0x0c030c02\n\t"
      "s_add_u32 s72, s24, 2564096\n\t"
      "s_addc_u32 s73, s25, 0\n\t"
      "s_waitcnt vmcnt(0)\n\t"
      "s_mov_b64 exec, 1\n\t"
      "v_mov_b32 v88, 1\n\t"
      "ds_add_rtn_u32 v88, v99, v88 offset:8\n\t"
      "s_waitcnt lgkmcnt(0)\n\t"
      "v_readfirstlane_b32 s78, v88\n\t"
      "s_mov_b64 exec, -1\n\t"
      "s_cmp_lg_u32 s78, 3\n\t"
      "s_cbranch_scc1 Le1_%=\n\t"
      "s_cmp_ge_u32 %[r], 128\n\t"
      "s_cbranch_scc1 Le0_%=\n\t"
      "Law5_%=:\n\t"
      "ds_read_b32 v103, v99 offset:48\n\t"
      "s_waitcnt lgkmcnt(0)\n\t"
      "v_cmp_ne_u32 vcc, 4, v103\n\t"
      "s_cbranch_vccnz Law5_%=\n\t"
      "global_store_dwordx4 v58, v[240:243], s[86:87] offset:0 sc1\n\t"
      "global_store_dwordx4 v58, v[244:247], s[86:87] offset:1024 sc1\n\t"
      "s_branch Le1_%=\n\t"
      "Le0_%=:\n\t"
      "Law6_%=:\n\t"
      "ds_read_b32 v103, v99 offset:44\n\t"
      "s_waitcnt lgkmcnt(0)\n\t"
      "v_cmp_ne_u32 vcc, 4, v103\n\t"
      "s_cbranch_vccnz Law6_%=\n\t"
      "v_add_u32 v88, 1, v125\n\t"
      "s_mov_b64 exec, 1\n\t"
      "global_store_dword v126, v88, s[86:87] sc1\n\t"
      "s_mov_b64 exec, -1\n\t"
      "Le1_%=:\n\t"
      "s_setprio 2\n\t"
      "s_movk_i32 s99, 400\n\t"
      "Lplt_%=:\n\t"
      "global_load_dwordx2 v[68:69], v94, s[84:85] sc1\n\t"
      "s_waitcnt vmcnt(0)\n\t"
      "v_cmp_eq_u32_sdwa vcc, v69, v98 src0_sel:WORD_1 src1_sel:WORD_1\n\t"
      "s_mov_b64 exec, vcc\n\t"
      "s_cbranch_execz Lpdt_%=\n\t"
      "s_sub_u32 s99, s99, 1\n\t"
      "s_sleep 1\n\t"
      "s_cmp_lg_u32 s99, 0\n\t"
      "s_cbranch_scc1 Lplt_%=\n\t"
      "v_mov_b32 v88, 1\n\t"
      "ds_write_b32 v99, v88 offset:16\n\t"
      "s_mov_b32 s98, 1\n\t"
      "Lpdt_%=:\n\t"
      "s_mov_b64 exec, -1\n\t"
      "v_sad_u8 v91, v68, 0, 0\n\t"
      "v_add_u32_sdwa v88, v69, v82 dst_sel:DWORD dst_unused:UNUSED_PAD src0_sel:WORD_0 src1_sel:DWORD\n\t"
      "v_lshl_add_u32 v70, v88, 4, v84\n\t"
      "v_sub_u32 v71, v91, v82\n\t"
      "v_cmp_lt_u32 vcc, 24, v91\n\t"
      "v_cmp_lt_i32 s[50:51], 0, v71\n\t"
      "v_cmp_lt_i32 s[52:53], 2, v71\n\t"
      "v_cmp_lt_i32 s[54:55], 4, v71\n\t"
      "v_cmp_lt_i32 s[56:57], 6, v71\n\t"
      "v_cmp_lt_i32 s[58:59], 8, v71\n\t"
      "v_cmp_lt_i32 s[60:61], 10, v71\n\t"
      "s_cmp_lg_u32 s98, 0\n\t"
      "s_cbranch_scc1 Lebd1_%=\n\t"
      "s_cbranch_vccnz Lebd1_%=\n\t"
      "s_mov_b64 exec, s[50:51]\n\t"
      "global_load_dwordx4 v[128:131], v70, s[72:73] offset:0 sc1\n\t"
      "s_mov_b64 exec, s[52:53]\n\t"
      "global_load_dwordx4 v[132:135], v70, s[72:73] offset:32 sc1\n\t"
      "s_mov_b64 exec, s[54:55]\n\t"
      "global_load_dwordx4 v[136:139], v70, s[72:73] offset:64 sc1\n\t"
      "s_mov_b64 exec, s[56:57]\n\t"
      "global_load_dwordx4 v[140:143], v70, s[72:73] offset:96 sc1\n\t"
      "s_mov_b64 exec, s[58:59]\n\t"
      "s_cbranch_execz Lr_%=\n\t"
      "global_load_dwordx4 v[144:147], v70, s[72:73] offset:128 sc1\n\t"
      "s_mov_b64 exec, s[60:61]\n\t"
      "s_cbranch_execz Lr_%=\n\t"
      "global_load_dwordx4 v[148:151], v70, s[72:73] offset:160 sc1\n\t"
      "v_cmp_lt_i32 vcc, 12, v71\n\t"
      "s_mov_b64 exec, vcc\n\t"
      "s_cbranch_execz Lr_%=\n\t"
      "global_load_dwordx4 v[152:155], v70, s[72:73] offset:192 sc1\n\t"
      "v_cmp_lt_i32 vcc, 14, v71\n\t"
      "s_mov_b64 exec, vcc\n\t"
      "s_cbranch_execz Lr_%=\n\t"
      "global_load_dwordx4 v[156:159], v70, s[72:73] offset:224 sc1\n\t"
      "v_cmp_lt_i32 vcc, 16, v71\n\t"
      "s_mov_b64 exec, vcc\n\t"
      "s_cbranch_execz Lr_%=\n\t"
      "global_load_dwordx4 v[160:163], v70, s[72:73] offset:256 sc1\n\t"
      "v_cmp_lt_i32 vcc, 18, v71\n\t"
      "s_mov_b64 exec, vcc\n\t"
      "s_cbranch_execz Lr_%=\n\t"
      "global_load_dwordx4 v[164:167], v70, s[72:73] offset:288 sc1\n\t"
      "v_cmp_lt_i32 vcc, 20, v71\n\t"
      "s_mov_b64 exec, vcc\n\t"
      "s_cbranch_execz Lr_%=\n\t"
      "global_load_dwordx4 v[168:171], v70, s[72:73] offset:320 sc1\n\t"
      "v_cmp_lt_i32 vcc, 22, v71\n\t"
      "s_mov_b64 exec, vcc\n\t"
      "s_cbranch_execz Lr_%=\n\t"
      "global_load_dwordx4 v[172:175], v70, s[72:73] offset:352 sc1\n\t"
      "Lr_%=:\n\t"
      "s_mov_b64 exec, 1\n\t"
      "ds_add_u32 v99, v127 offset:52\n\t"
      "s_mov_b64 exec, -1\n\t"
      "v_perm_b32 v72, 0, v68, s74\n\t"
      "v_perm_b32 v73, 0, v68, s75\n\t"
      "s_nop 0\n\t"
      "v_add_u32_dpp v74, v72, v72 row_shr:2 row_mask:0xf bank_mask:0xf bound_ctrl:1\n\t"
      "v_add_u32_dpp v75, v73, v73 row_shr:2 row_mask:0xf bank_mask:0xf bound_ctrl:1\n\t"
      "s_nop 0\n\t"
      "v_add_u32_dpp v74, v74, v74 row_shr:4 row_mask:0xf bank_mask:0xf bound_ctrl:1\n\t"
      "v_add_u32_dpp v75, v75, v75 row_shr:4 row_mask:0xf bank_mask:0xf bound_ctrl:1\n\t"
      "s_nop 0\n\t"
      "v_add_u32_dpp v74, v74, v74 row_shr:8 row_mask:0xf bank_mask:0xf bound_ctrl:1\n\t"
      "v_add_u32_dpp v75, v75, v75 row_shr:8 row_mask:0xf bank_mask:0xf bound_ctrl:1\n\t"
      "s_nop 0\n\t"
      "v_add_u32_dpp v74, v74, v74 row_bcast:15 row_mask:0xa bank_mask:0xf\n\t"
      "v_add_u32_dpp v75, v75, v75 row_bcast:15 row_mask:0xa bank_mask:0xf\n\t"
      "s_nop 0\n\t"
      "v_add_u32_dpp v74, v74, v74 row_bcast:31 row_mask:0xc bank_mask:0xf\n\t"
      "v_add_u32_dpp v75, v75, v75 row_bcast:31 row_mask:0xc bank_mask:0xf\n\t"
      "v_mov_b32 v76, v74\n\t"
      "v_or_b32 v77, 2.0, v75\n\t"
      "s_brev_b64 exec, 1\n\t"
      "ds_add_rtn_u64 v[80:81], v99, v[76:77]\n\t"
      "s_mov_b64 exec, -1\n\t"
      "v_sub_u32 v72, v74, v72\n\t"
      "v_sub_u32 v73, v75, v73\n\t"
      "v_readlane_b32 s74, v74, 63\n\t"
      "v_readlane_b32 s75, v75, 63\n\t"
      "s_waitcnt lgkmcnt(0)\n\t"
      "v_readlane_b32 s78, v80, 63\n\t"
      "v_readlane_b32 s93, v81, 63\n\t"
      "s_and_b32 s79, s93, 0x3fffffff\n\t"
      "s_add_u32 s62, s74, s78\n\t"
      "s_add_u32 s63, s75, s79\n\t"
      "s_add_u32 s74, s62, 2132770591\n\t"
      "s_add_u32 s75, s63, 2132770591\n\t"
      "s_or_b32 s74, s74, s75\n\t"
      "s_and_b32 s74, s74, 0x80008000\n\t"
      "s_cbranch_scc1 Lebad_%=\n\t"
      "s_add_u32 s78, s78, 14680064\n\t"
      "s_add_u32 s79, s79, 44040640\n\t"
      "v_add_lshl_u32 v76, v72, s78, 5\n\t"
      "v_add_lshl_u32 v77, v73, s79, 5\n\t"
      "s_lshr_b32 s93, s93, 30\n\t"
      "s_cmp_lg_u32 s93, 3\n\t"
      "s_cbranch_scc1 Lnl_%=\n\t"
      "s_and_b32 s70, s62, 0xffff\n\t"
      "s_lshr_b32 s71, s62, 16\n\t"
      "s_and_b32 s72, s63, 0xffff\n\t"
      "s_lshr_b32 s73, s63, 16\n\t"
      "s_add_u32 s74, s70, s71\n\t"
      "s_add_u32 s75, s72, s73\n\t"
      "s_absdiff_i32 s74, s74, s75\n\t"
      "s_add_u32 s75, s70, s72\n\t"
      "s_add_u32 s76, s71, s73\n\t"
      "s_absdiff_i32 s75, s75, s76\n\t"
      "s_add_u32 s76, s70, s73\n\t"
      "s_add_u32 s77, s71, s72\n\t"
      "s_absdiff_i32 s76, s76, s77\n\t"
      "s_add_u32 s78, s70, 7\n\t"
      "s_lshr_b32 s78, s78, 3\n\t"
      "s_and_b32 s79, s78, 3\n\t"
      "s_cmp_eq_u32 s79, 3\n\t"
      "s_addc_u32 s78, s78, 0\n\t"
      "s_lshl_b32 s70, s70, 6\n\t"
      "s_add_u32 s70, s70, s78\n\t"
      "s_lshl2_add_u32 s70, s70, 0\n\t"
      "s_add_u32 s78, s71, 7\n\t"
      "s_lshr_b32 s78, s78, 3\n\t"
      "s_and_b32 s79, s78, 3\n\t"
      "s_cmp_eq_u32 s79, 3\n\t"
      "s_addc_u32 s78, s78, 0\n\t"
      "s_lshl_b32 s71, s71, 6\n\t"
      "s_add_u32 s71, s71, s78\n\t"
      "s_lshl2_add_u32 s71, s71, 1\n\t"
      "s_add_u32 s78, s72, 7\n\t"
      "s_lshr_b32 s78, s78, 3\n\t"
      "s_and_b32 s79, s78, 3\n\t"
      "s_cmp_eq_u32 s79, 3\n\t"
      "s_addc_u32 s78, s78, 0\n\t"
      "s_lshl_b32 s72, s72, 6\n\t"
      "s_add_u32 s72, s72, s78\n\t"
      "s_lshl2_add_u32 s72, s72, 2\n\t"
      "s_add_u32 s78, s73, 7\n\t"
      "s_lshr_b32 s78, s78, 3\n\t"
      "s_and_b32 s79, s78, 3\n\t"
      "s_cmp_eq_u32 s79, 3\n\t"
      "s_addc_u32 s78, s78, 0\n\t"
      "s_lshl_b32 s73, s73, 6\n\t"
      "s_add_u32 s73, s73, s78\n\t"
      "s_lshl2_add_u32 s73, s73, 3\n\t"
      "s_cmp_le_u32 s75, s76\n\t"
      "s_cselect_b32 s78, s72, s73\n\t"
      "s_cselect_b32 s79, s73, s72\n\t"
      "s_min_u32 s75, s75, s76\n\t"
      "s_cmp_le_u32 s74, s75\n\t"
      "s_cselect_b32 s78, s71, s78\n\t"
      "s_cselect_b32 s79, s73, s79\n\t"
      "s_cselect_b32 s93, s72, s71\n\t"
      "s_max_u32 s74, s70, s78\n\t"
      "s_min_u32 s76, s70, s78\n\t"
      "s_max_u32 s75, s93, s79\n\t"
      "s_min_u32 s77, s93, s79\n\t"
      "s_pack_ll_b32_b16 s78, s74, s75\n\t"
      "s_pack_ll_b32_b16 s79, s76, s77\n\t"
      "v_mov_b32 v80, s78\n\t"
      "v_mov_b32 v81, s79\n\t"
      "ds_write2_b32 v99, v80, v81 offset0:5 offset1:6\n\t"
      "Lnl_%=:\n\t"
      "s_waitcnt vmcnt(0)\n\t"
      "s_mov_b64 exec, s[50:51]\n\t"
      "v_lshrrev_b64 v[80:81], v131, v[76:77]\n\t"
      "v_add_u32_sdwa v78, v80, v131 dst_sel:DWORD dst_unused:UNUSED_PAD src0_sel:WORD_0 src1_sel:WORD_1\n\t"
      "ds_write_b16 v78, v130 offset:20\n\t"
      "v_add_u32_sdwa v79, v78, v130 dst_sel:DWORD dst_unused:UNUSED_PAD src0_sel:DWORD src1_sel:WORD_1\n\t"
      "ds_write_b64 v78, v[128:129] offset:24\n\t"
      "ds_write_b16 v79, v85\n\t"
      "s_mov_b64 exec, s[52:53]\n\t"
      "v_lshrrev_b64 v[80:81], v135, v[76:77]\n\t"
      "v_add_u32_sdwa v78, v80, v135 dst_sel:DWORD dst_unused:UNUSED_PAD src0_sel:WORD_0 src1_sel:WORD_1\n\t"
      "ds_write_b16 v78, v134 offset:20\n\t"
      "v_add_u32_sdwa v79, v78, v134 dst_sel:DWORD dst_unused:UNUSED_PAD src0_sel:DWORD src1_sel:WORD_1\n\t"
      "ds_write_b64 v78, v[132:133] offset:24\n\t"
      "ds_write_b16 v79, v85\n\t"
      "s_mov_b64 exec, s[54:55]\n\t"
      "v_lshrrev_b64 v[80:81], v139, v[76:77]\n\t"
      "v_add_u32_sdwa v78, v80, v139 dst_sel:DWORD dst_unused:UNUSED_PAD src0_sel:WORD_0 src1_sel:WORD_1\n\t"
      "ds_write_b16 v78, v138 offset:20\n\t"
      "v_add_u32_sdwa v79, v78, v138 dst_sel:DWORD dst_unused:UNUSED_PAD src0_sel:DWORD src1_sel:WORD_1\n\t"
      "ds_write_b64 v78, v[136:137] offset:24\n\t"
      "ds_write_b16 v79, v85\n\t"
      "s_mov_b64 exec, s[56:57]\n\t"
      "v_lshrrev_b64 v[80:81], v143, v[76:77]\n\t"
      "v_add_u32_sdwa v78, v80, v143 dst_sel:DWORD dst_unused:UNUSED_PAD src0_sel:WORD_0 src1_sel:WORD_1\n\t"
      "ds_write_b16 v78, v142 offset:20\n\t"
      "v_add_u32_sdwa v79, v78, v142 dst_sel:DWORD dst_unused:UNUSED_PAD src0_sel:DWORD src1_sel:WORD_1\n\t"
      "ds_write_b64 v78, v[140:141] offset:24\n\t"
      "ds_write_b16 v79, v85\n\t"
      "s_mov_b64 exec, s[58:59]\n\t"
      "s_cbranch_execz Ljoin_%=\n\t"
      "v_lshrrev_b64 v[80:81], v147, v[76:77]\n\t"
      "v_add_u32_sdwa v78, v80, v147 dst_sel:DWORD dst_unused:UNUSED_PAD src0_sel:WORD_0 src1_sel:WORD_1\n\t"
      "ds_write_b16 v78, v146 offset:20\n\t"
      "v_add_u32_sdwa v79, v78, v146 dst_sel:DWORD dst_unused:UNUSED_PAD src0_sel:DWORD src1_sel:WORD_1\n\t"
      "ds_write_b64 v78, v[144:145] offset:24\n\t"
      "ds_write_b16 v79, v85\n\t"
      "s_mov_b64 exec, s[60:61]\n\t"
      "s_cbranch_execz Ljoin_%=\n\t"
      "v_lshrrev_b64 v[80:81], v151, v[76:77]\n\t"
      "v_add_u32_sdwa v78, v80, v151 dst_sel:DWORD dst_unused:UNUSED_PAD src0_sel:WORD_0 src1_sel:WORD_1\n\t"
      "ds_write_b16 v78, v150 offset:20\n\t"
      "v_add_u32_sdwa v79, v78, v150 dst_sel:DWORD dst_unused:UNUSED_PAD src0_sel:DWORD src1_sel:WORD_1\n\t"
      "ds_write_b64 v78, v[148:149] offset:24\n\t"
      "ds_write_b16 v79, v85\n\t"
      "v_cmp_lt_i32 vcc, 12, v71\n\t"
      "s_mov_b64 exec, vcc\n\t"
      "s_cbranch_execz Ljoin_%=\n\t"
      "v_lshrrev_b64 v[80:81], v155, v[76:77]\n\t"
      "v_add_u32_sdwa v78, v80, v155 dst_sel:DWORD dst_unused:UNUSED_PAD src0_sel:WORD_0 src1_sel:WORD_1\n\t"
      "ds_write_b16 v78, v154 offset:20\n\t"
      "v_add_u32_sdwa v79, v78, v154 dst_sel:DWORD dst_unused:UNUSED_PAD src0_sel:DWORD src1_sel:WORD_1\n\t"
      "ds_write_b64 v78, v[152:153] offset:24\n\t"
      "ds_write_b16 v79, v85\n\t"
      "v_cmp_lt_i32 vcc, 14, v71\n\t"
      "s_mov_b64 exec, vcc\n\t"
      "s_cbranch_execz Ljoin_%=\n\t"
      "v_lshrrev_b64 v[80:81], v159, v[76:77]\n\t"
      "v_add_u32_sdwa v78, v80, v159 dst_sel:DWORD dst_unused:UNUSED_PAD src0_sel:WORD_0 src1_sel:WORD_1\n\t"
      "ds_write_b16 v78, v158 offset:20\n\t"
      "v_add_u32_sdwa v79, v78, v158 dst_sel:DWORD dst_unused:UNUSED_PAD src0_sel:DWORD src1_sel:WORD_1\n\t"
      "ds_write_b64 v78, v[156:157] offset:24\n\t"
      "ds_write_b16 v79, v85\n\t"
      "v_cmp_lt_i32 vcc, 16, v71\n\t"
      "s_mov_b64 exec, vcc\n\t"
      "s_cbranch_execz Ljoin_%=\n\t"
      "v_lshrrev_b64 v[80:81], v163, v[76:77]\n\t"
      "v_add_u32_sdwa v78, v80, v163 dst_sel:DWORD dst_unused:UNUSED_PAD src0_sel:WORD_0 src1_sel:WORD_1\n\t"
      "ds_write_b16 v78, v162 offset:20\n\t"
      "v_add_u32_sdwa v79, v78, v162 dst_sel:DWORD dst_unused:UNUSED_PAD src0_sel:DWORD src1_sel:WORD_1\n\t"
      "ds_write_b64 v78, v[160:161] offset:24\n\t"
      "ds_write_b16 v79, v85\n\t"
      "v_cmp_lt_i32 vcc, 18, v71\n\t"
      "s_mov_b64 exec, vcc\n\t"
      "s_cbranch_execz Ljoin_%=\n\t"
      "v_lshrrev_b64 v[80:81], v167, v[76:77]\n\t"
      "v_add_u32_sdwa v78, v80, v167 dst_sel:DWORD dst_unused:UNUSED_PAD src0_sel:WORD_0 src1_sel:WORD_1\n\t"
      "ds_write_b16 v78, v166 offset:20\n\t"
      "v_add_u32_sdwa v79, v78, v166 dst_sel:DWORD dst_unused:UNUSED_PAD src0_sel:DWORD src1_sel:WORD_1\n\t"
      "ds_write_b64 v78, v[164:165] offset:24\n\t"
      "ds_write_b16 v79, v85\n\t"
      "v_cmp_lt_i32 vcc, 20, v71\n\t"
      "s_mov_b64 exec, vcc\n\t"
      "s_cbranch_execz Ljoin_%=\n\t"
      "v_lshrrev_b64 v[80:81], v171, v[76:77]\n\t"
      "v_add_u32_sdwa v78, v80, v171 dst_sel:DWORD dst_unused:UNUSED_PAD src0_sel:WORD_0 src1_sel:WORD_1\n\t"
      "ds_write_b16 v78, v170 offset:20\n\t"
      "v_add_u32_sdwa v79, v78, v170 dst_sel:DWORD dst_unused:UNUSED_PAD src0_sel:DWORD src1_sel:WORD_1\n\t"
      "ds_write_b64 v78, v[168:169] offset:24\n\t"
      "ds_write_b16 v79, v85\n\t"
      "v_cmp_lt_i32 vcc, 22, v71\n\t"
      "s_mov_b64 exec, vcc\n\t"
      "s_cbranch_execz Ljoin_%=\n\t"
      "v_lshrrev_b64 v[80:81], v175, v[76:77]\n\t"
      "v_add_u32_sdwa v78, v80, v175 dst_sel:DWORD dst_unused:UNUSED_PAD src0_sel:WORD_0 src1_sel:WORD_1\n\t"
      "ds_write_b16 v78, v174 offset:20\n\t"
      "v_add_u32_sdwa v79, v78, v174 dst_sel:DWORD dst_unused:UNUSED_PAD src0_sel:DWORD src1_sel:WORD_1\n\t"
      "ds_write_b64 v78, v[172:173] offset:24\n\t"
      "ds_write_b16 v79, v85\n\t"
      "s_branch Ljoin_%=\n\t"
      "Lebd1_%=:\n\t"
      "s_mov_b64 exec, 1\n\t"
      "ds_add_u32 v99, v127 offset:52\n\t"
      "s_mov_b64 exec, -1\n\t"
      "Lebad_%=:\n\t"
      "v_mov_b32 v88, 1\n\t"
      "ds_write_b32 v99, v88 offset:16\n\t"
      "s_branch Ljoin_%=\n\t"
      "Lbk_%=:\n\t"
      "s_cmp_lg_u32 s48, 7\n\t"
      "s_cbranch_scc1 Lb3_%=\n\t"
      "s_getpc_b64 s[70:71]\n\t"
      "Lpc_%=:\n\t"
      "s_sub_u32 s70, s70, Lpc_%=-Lst_%=\n\t"
      "s_subb_u32 s71, s71, 0\n\t"
      "s_lshr_b32 s78, %[r], 3\n\t"
      "s_mul_i32 s78, s78, 448\n\t"
      "v_and_b32 v88, 3, v0\n\t"
      "v_lshl_add_u32 v88, v88, 7, s78\n\t"
      "s_mov_b64 exec, 15\n\t"
      "global_load_dword v68, v88, s[70:71]\n\t"
      "s_mov_b64 exec, -1\n\t"
      "Lb3_%=:\n\t"
      "v_and_b32 v97, 0xff, v0\n\t"
      "v_bfe_i32 v91, v0, 0, 1\n\t"
      "v_mov_b64 v[130:131], 0\n\t"
      "v_mov_b32 v128, 0\n\t"
      "v_bfe_u32 v126, v0, 1, 7\n\t"
      "v_lshlrev_b32 v97, 4, v97\n\t"
      "v_and_b32 v129, 0x7000, v91\n\t"
      "v_and_b32 v90, 15, v0\n\t"
      "v_lshlrev_b32 v90, 2, v90\n\t"
      "ds_write_b32 v90, v128 offset:32768\n\t"
      "ds_write_b128 v97, v[128:131] offset:0\n\t"
      "ds_write_b128 v97, v[128:131] offset:4096\n\t"
      "ds_write_b128 v97, v[128:131] offset:8192\n\t"
      "ds_write_b128 v97, v[128:131] offset:12288\n\t"
      "ds_write_b128 v97, v[128:131] offset:16384\n\t"
      "ds_write_b128 v97, v[128:131] offset:20480\n\t"
      "ds_write_b128 v97, v[128:131] offset:24576\n\t"
      "ds_write_b128 v97, v[128:131] offset:28672\n\t"
      "v_and_b32 v86, 31, v0\n\t"
      "v_bfe_u32 v87, v0, 5, 1\n\t"
      "v_lshrrev_b32 v93, 4, v0\n\t"
      "v_bfe_u32 v92, v0, 4, 2\n\t"
      "v_and_b32 v58, 63, v0\n\t"
      "v_lshlrev_b32 v58, 4, v58\n\t"
      "s_and_b32 s78, %[b], 7\n\t"
      "s_lshl_b32 s78, s78, 5\n\t"
      "s_lshr_b32 s79, %[b], 3\n\t"
      "s_add_u32 s78, s78, s79\n\t"
      "s_lshl_b32 s92, s78, 8\n\t"
      "s_mov_b32 s98, 0\n\t"
      "s_mov_b32 s80, 0xbfb8aa3b\n\t"
      "s_mov_b32 s81, 0xbfb8aa3b\n\t"
      "v_mov_b32 v99, 32768\n\t"
      "v_mov_b32 v127, 1\n\t"
      "s_and_b32 s77, s48, 3\n\t"
      "v_lshlrev_b32 v126, 7, v126\n\t"
      "v_and_b32 v91, 15, v0\n\t"
      "v_lshlrev_b32 v90, 2, v92\n\t"
      "v_xor_b32 v90, v91, v90\n\t"
      "v_lshlrev_b32 v90, 4, v90\n\t"
      "v_lshl_or_b32 v252, v92, 8, v90\n\t"
      "v_xor_b32 v253, 16, v252\n\t"
      "v_xor_b32 v254, 32, v252\n\t"
      "v_xor_b32 v255, 48, v252\n\t"
      "v_and_b32 v88, 15, v93\n\t"
      "v_xor_b32 v90, v88, v91\n\t"
      "v_lshlrev_b32 v90, 4, v90\n\t"
      "v_lshl_or_b32 v104, v88, 16, v90\n\t"
      "v_lshlrev_b32 v106, 1, v86\n\t"
      "v_mov_b64 v[116:117], 0\n\t"
      "v_mov_b64 v[118:119], 0\n\t"
      "s_mov_b32 s30, 2560000\n\t"
      "s_mov_b32 s31, 0x20000\n\t"
      "s_lshl_b32 s50, s77, 10\n\t"
      "s_add_u32 s58, s92, 1048576\n\t"
      "s_add_u32 s60, s92, 2097152\n\t"
      "s_add_u32 m0, s50, 33024\n\t"
      "s_waitcnt lgkmcnt(0)\n\t"
      "s_mov_b32 s28, s12\n\t"
      "s_mov_b32 s29, s13\n\t"
      "s_add_u32 s84, s24, 5398528\n\t"
      "s_addc_u32 s85, s25, 0\n\t"
      "s_lshl_b32 s76, s77, 2\n\t"
      "v_bfe_u32 v90, v0, 2, 2\n\t"
      "v_lshl_or_b32 v89, v87, 3, v90\n\t"
      "v_lshlrev_b32 v90, 2, v90\n\t"
      "v_lshl_or_b32 v90, v87, 1, v90\n\t"
      "v_bfe_u32 v91, v0, 1, 1\n\t"
      "v_and_b32 v88, 16, v0\n\t"
      "v_lshrrev_b32 v88, 3, v88\n\t"
      "v_or3_b32 v91, v91, v88, s76\n\t"
      "v_xor_b32 v90, v90, v91\n\t"
      "v_lshlrev_b32 v90, 4, v90\n\t"
      "v_lshl_or_b32 v90, v89, 8, v90\n\t"
      "v_and_b32 v88, 1, v0\n\t"
      "v_lshl_or_b32 v90, v88, 3, v90\n\t"
      "v_add_u32 v109, 59648, v90\n\t"
      "v_xor_b32 v90, 16, v90\n\t"
      "v_add_u32 v110, 60672, v90\n\t"
      "v_lshrrev_b32 v90, 1, v86\n\t"
      "v_xor_b32 v90, v90, v86\n\t"
      "v_and_b32 v90, 4, v90\n\t"
      "v_mul_u32_u24 v90, 3, v90\n\t"
      "v_xor_b32 v90, v90, v86\n\t"
      "v_and_b32 v90, 15, v90\n\t"
      "v_min_u32 v90, 9, v90\n\t"
      "v_lshrrev_b32 v89, 4, v86\n\t"
      "v_mad_u32_u24 v108, v89, 10, v90\n\t"
      "s_barrier\n\t"
      "v_add_u32 v88, 0, v108\n\t"
      "v_and_b32 v89, 15, v88\n\t"
      "v_xor_b32 v89, v89, v87\n\t"
      "v_lshlrev_b32 v89, 4, v89\n\t"
      "v_lshl_or_b32 v224, v88, 8, v89\n\t"
      "v_xor_b32 v225, 32, v224\n\t"
      "v_xor_b32 v226, 64, v224\n\t"
      "v_xor_b32 v227, 96, v224\n\t"
      "v_xor_b32 v228, 128, v224\n\t"
      "v_xor_b32 v229, 160, v224\n\t"
      "v_xor_b32 v230, 192, v224\n\t"
      "v_xor_b32 v231, 224, v224\n\t"
      "v_add_u32 v88, 20, v108\n\t"
      "v_and_b32 v89, 15, v88\n\t"
      "v_xor_b32 v89, v89, v87\n\t"
      "v_lshlrev_b32 v89, 4, v89\n\t"
      "v_lshl_or_b32 v232, v88, 8, v89\n\t"
      "v_xor_b32 v233, 32, v232\n\t"
      "v_xor_b32 v234, 64, v232\n\t"
      "v_xor_b32 v235, 96, v232\n\t"
      "v_xor_b32 v236, 128, v232\n\t"
      "v_xor_b32 v237, 160, v232\n\t"
      "v_xor_b32 v238, 192, v232\n\t"
      "v_xor_b32 v239, 224, v232\n\t"
      "v_mov_b32 v85, 0x3f80\n\t"
      "v_and_b32 v96, 7, v0\n\t"
      "v_xor_b32 v89, v92, v96\n\t"
      "s_and_b32 s95, s48, 1\n\t"
      "s_lshl_b32 s95, s95, 7\n\t"
      "v_lshl_or_b32 v95, v89, 4, s95\n\t"
      "v_bfe_u32 v88, v0, 3, 3\n\t"
      "v_lshl_or_b32 v57, v88, 5, 20\n\t"
      "s_lshl_b32 s78, s48, 13\n\t"
      "s_add_u32 s42, s78, 92416\n\t"
      "v_and_b32 v88, 1, v86\n\t"
      "v_lshlrev_b32 v88, 4, v88\n\t"
      "v_lshlrev_b32 v88, v88, v85\n\t"
      "v_bfe_u32 v89, v86, 1, 1\n\t"
      "v_and_b32 v90, 4, v86\n\t"
      "v_bfe_u32 v91, v86, 3, 1\n\t"
      "v_lshl_or_b32 v89, v91, 1, v89\n\t"
      "v_lshrrev_b32 v91, 4, v86\n\t"
      "v_xor_b32 v91, v91, v87\n\t"
      "v_lshlrev_b32 v91, 3, v91\n\t"
      "v_or3_b32 v89, v89, v90, v91\n\t"
      "v_cmp_eq_u32 s[62:63], 0, v89\n\t"
      "v_cmp_eq_u32 s[64:65], 1, v89\n\t"
      "v_cmp_eq_u32 s[66:67], 2, v89\n\t"
      "v_cmp_eq_u32 s[68:69], 3, v89\n\t"
      "v_cndmask_b32 v42, 0, v88, s[62:63]\n\t"
      "v_cndmask_b32 v43, 0, v88, s[64:65]\n\t"
      "v_cndmask_b32 v44, 0, v88, s[66:67]\n\t"
      "v_cndmask_b32 v45, 0, v88, s[68:69]\n\t"
      "v_cmp_eq_u32 s[62:63], 4, v89\n\t"
      "v_cmp_eq_u32 s[64:65], 5, v89\n\t"
      "v_cmp_eq_u32 s[66:67], 6, v89\n\t"
      "v_cmp_eq_u32 s[68:69], 7, v89\n\t"
      "v_cndmask_b32 v46, 0, v88, s[62:63]\n\t"
      "v_cndmask_b32 v47, 0, v88, s[64:65]\n\t"
      "v_cndmask_b32 v48, 0, v88, s[66:67]\n\t"
      "v_cndmask_b32 v49, 0, v88, s[68:69]\n\t"
      "v_bfe_u32 v88, v0, 1, 3\n\t"
      "v_xor_b32 v88, v88, v87\n\t"
      "v_lshlrev_b32 v88, 4, v88\n\t"
      "v_lshl_add_u32 v90, v86, 7, s42\n\t"
      "v_add_u32 v50, v88, v90\n\t"
      "v_xor_b32 v51, 32, v50\n\t"
      "v_xor_b32 v52, 64, v50\n\t"
      "v_xor_b32 v53, 0x60, v50\n\t"
      "v_lshlrev_b32 v88, 4, v87\n\t"
      "v_lshl_add_u32 v54, v86, 5, v88\n\t"
      "v_and_b32 v88, 15, v0\n\t"
      "v_lshlrev_b32 v90, 3, v88\n\t"
      "v_lshl_or_b32 v55, v87, 7, v90\n\t"
      "v_lshlrev_b32 v91, 4, v88\n\t"
      "v_lshl_or_b32 v67, v93, 16, v91\n\t"
      "v_mul_u32_u24 v66, 272, v93\n\t"
      "v_add_u32 v91, 72704, v91\n\t"
      "v_add_u32 v66, v66, v91\n\t"
      "v_xor_b32 v91, v92, v88\n\t"
      "v_lshlrev_b32 v91, 4, v91\n\t"
      "v_lshl_or_b32 v60, v88, 8, v91\n\t"
      "v_xor_b32 v61, 64, v60\n\t"
      "v_xor_b32 v62, 0x80, v60\n\t"
      "v_xor_b32 v63, 0xc0, v60\n\t"
      "v_mul_u32_u24 v90, 272, v88\n\t"
      "v_add_u32 v90, 59648, v90\n\t"
      "v_lshl_add_u32 v64, v92, 4, v90\n\t"
      "v_lshl_add_u32 v65, v92, 3, v90\n\t"
      "s_lshl_b32 s79, s48, 5\n\t"
      "v_add_u32 v65, s79, v65\n\t"
      "v_mul_u32_u24 v88, 272, v86\n\t"
      "v_lshl_add_u32 v88, v87, 3, v88\n\t"
      "s_add_u32 s78, s95, 59648\n\t"
      "v_add_u32 v59, s78, v88\n\t"
      "v_cmp_gt_u32 s[96:97], 10, v86\n\t"
      "Law7_%=:\n\t"
      "ds_read_b32 v103, v99 offset:32\n\t"
      "s_waitcnt lgkmcnt(0)\n\t"
      "v_cmp_ne_u32 vcc, 4, v103\n\t"
      "s_cbranch_vccnz Law7_%=\n\t"
      "global_load_dword v98, v126, s[84:85] sc1\n\t"
      "buffer_load_dwordx4 v104, s[28:31], s92 offen lds\n\t"
      "s_add_u32 m0, s50, 37120\n\t"
      "s_cmp_gt_u32 s48, 5\n\t"
      "buffer_load_dwordx4 v104, s[28:31], s58 offen lds\n\t"
      "s_cbranch_scc1 Lb1_%=\n\t"
      "s_add_u32 m0, s50, 41216\n\t"
      "s_nop 0\n\t"
      "buffer_load_dwordx4 v104, s[28:31], s60 offen lds\n\t"
      "Lb1_%=:\n\t"
      "s_lshl_b32 s79, s77, 6\n\t"
      "s_add_u32 s79, s79, 65536\n\t"
      "s_add_u32 s70, s20, s79\n\t"
      "s_addc_u32 s71, s21, 0\n\t"
      "global_load_short_d16_hi v116, v106, s[70:71] offset:0\n\t"
      "global_load_short_d16_hi v117, v106, s[70:71] offset:256\n\t"
      "global_load_short_d16_hi v118, v106, s[70:71] offset:512\n\t"
      "global_load_short_d16_hi v119, v106, s[70:71] offset:768\n\t"
      "s_cmp_ge_u32 %[r], 128\n\t"
      "s_cbranch_scc1 Lbl_%=\n\t"
      "s_waitcnt vmcnt(0)\n\t"
      "s_mov_b64 exec, 1\n\t"
      "ds_add_u32 v99, v127 offset:48\n\t"
      "s_mov_b64 exec, -1\n\t"
      "Law8_%=:\n\t"
      "ds_read_b32 v103, v99 offset:40\n\t"
      "s_waitcnt lgkmcnt(0)\n\t"
      "v_cmp_ne_u32 vcc, 4, v103\n\t"
      "s_cbranch_vccnz Law8_%=\n\t"
      "Lbl_%=:\n\t"
      "s_lshl_b32 s79, s77, 13\n\t"
      "s_add_u32 s75, s79, 32768\n\t"
      "s_add_u32 s70, s20, s75\n\t"
      "s_addc_u32 s71, s21, 0\n\t"
      "s_add_u32 s72, s70, 4096\n\t"
      "s_addc_u32 s73, s71, 0\n\t"
      "s_add_u32 m0, s79, 59648\n\t"
      "s_nop 0\n\t"
      "global_load_lds_dwordx4 v252, s[70:71] offset:0\n\t"
      "global_load_lds_dwordx4 v253, s[70:71] offset:1024\n\t"
      "global_load_lds_dwordx4 v254, s[70:71] offset:2048\n\t"
      "global_load_lds_dwordx4 v255, s[70:71] offset:3072\n\t"
      "s_add_u32 m0, s79, 63744\n\t"
      "s_nop 0\n\t"
      "global_load_lds_dwordx4 v252, s[72:73] offset:0\n\t"
      "global_load_lds_dwordx4 v253, s[72:73] offset:1024\n\t"
      "global_load_lds_dwordx4 v254, s[72:73] offset:2048\n\t"
      "global_load_lds_dwordx4 v255, s[72:73] offset:3072\n\t"
      "s_waitcnt vmcnt(0)\n\t"
      "s_mov_b64 exec, 1\n\t"
      "ds_add_u32 v99, v127 offset:44\n\t"
      "s_mov_b64 exec, -1\n\t"
      "Law9_%=:\n\t"
      "ds_read_b32 v103, v99 offset:44\n\t"
      "s_waitcnt lgkmcnt(0)\n\t"
      "v_cmp_ne_u32 vcc, 4, v103\n\t"
      "s_cbranch_vccnz Law9_%=\n\t"
      "ds_read_b64_tr_b16 v[160:161], v109 offset:0\n\t"
      "ds_read_b64_tr_b16 v[162:163], v110 offset:0\n\t"
      "ds_read_b128 v[128:131], v224 offset:33024\n\t"
      "ds_read_b128 v[132:135], v232 offset:33024\n\t"
      "ds_read_b64_tr_b16 v[164:165], v109 offset:4096\n\t"
      "ds_read_b64_tr_b16 v[166:167], v110 offset:4096\n\t"
      "ds_read_b128 v[136:139], v225 offset:33024\n\t"
      "ds_read_b128 v[140:143], v233 offset:33024\n\t"
      "ds_read_b64_tr_b16 v[168:169], v109 offset:8192\n\t"
      "ds_read_b64_tr_b16 v[170:171], v110 offset:8192\n\t"
      "ds_read_b128 v[144:147], v226 offset:33024\n\t"
      "ds_read_b128 v[148:151], v234 offset:33024\n\t"
      "s_waitcnt lgkmcnt(8)\n\t"
      "v_mfma_f32_32x32x16_bf16 v[192:207], v[128:131], v[160:163], 0\n\t"
      "v_mfma_f32_32x32x16_bf16 v[208:223], v[132:135], v[160:163], 0\n\t"
      "ds_read_b64_tr_b16 v[172:173], v109 offset:12288\n\t"
      "ds_read_b64_tr_b16 v[174:175], v110 offset:12288\n\t"
      "ds_read_b128 v[152:155], v227 offset:33024\n\t"
      "ds_read_b128 v[156:159], v235 offset:33024\n\t"
      "s_waitcnt lgkmcnt(8)\n\t"
      "v_mfma_f32_32x32x16_bf16 v[192:207], v[136:139], v[164:167], v[192:207]\n\t"
      "v_mfma_f32_32x32x16_bf16 v[208:223], v[140:143], v[164:167], v[208:223]\n\t"
      "ds_read_b64_tr_b16 v[176:177], v109 offset:16384\n\t"
      "ds_read_b64_tr_b16 v[178:179], v110 offset:16384\n\t"
      "ds_read_b128 v[128:131], v228 offset:33024\n\t"
      "ds_read_b128 v[132:135], v236 offset:33024\n\t"
      "s_waitcnt lgkmcnt(8)\n\t"
      "v_mfma_f32_32x32x16_bf16 v[192:207], v[144:147], v[168:171], v[192:207]\n\t"
      "v_mfma_f32_32x32x16_bf16 v[208:223], v[148:151], v[168:171], v[208:223]\n\t"
      "ds_read_b64_tr_b16 v[180:181], v109 offset:20480\n\t"
      "ds_read_b64_tr_b16 v[182:183], v110 offset:20480\n\t"
      "ds_read_b128 v[136:139], v229 offset:33024\n\t"
      "ds_read_b128 v[140:143], v237 offset:33024\n\t"
      "s_waitcnt lgkmcnt(8)\n\t"
      "v_mfma_f32_32x32x16_bf16 v[192:207], v[152:155], v[172:175], v[192:207]\n\t"
      "v_mfma_f32_32x32x16_bf16 v[208:223], v[156:159], v[172:175], v[208:223]\n\t"
      "ds_read_b64_tr_b16 v[184:185], v109 offset:24576\n\t"
      "ds_read_b64_tr_b16 v[186:187], v110 offset:24576\n\t"
      "ds_read_b128 v[144:147], v230 offset:33024\n\t"
      "ds_read_b128 v[148:151], v238 offset:33024\n\t"
      "s_waitcnt lgkmcnt(8)\n\t"
      "v_mfma_f32_32x32x16_bf16 v[192:207], v[128:131], v[176:179], v[192:207]\n\t"
      "v_mfma_f32_32x32x16_bf16 v[208:223], v[132:135], v[176:179], v[208:223]\n\t"
      "ds_read_b64_tr_b16 v[188:189], v109 offset:28672\n\t"
      "ds_read_b64_tr_b16 v[190:191], v110 offset:28672\n\t"
      "ds_read_b128 v[152:155], v231 offset:33024\n\t"
      "ds_read_b128 v[156:159], v239 offset:33024\n\t"
      "s_waitcnt lgkmcnt(8)\n\t"
      "v_mfma_f32_32x32x16_bf16 v[192:207], v[136:139], v[180:183], v[192:207]\n\t"
      "v_mfma_f32_32x32x16_bf16 v[208:223], v[140:143], v[180:183], v[208:223]\n\t"
      "s_waitcnt lgkmcnt(4)\n\t"
      "v_mfma_f32_32x32x16_bf16 v[192:207], v[144:147], v[184:187], v[192:207]\n\t"
      "v_mfma_f32_32x32x16_bf16 v[208:223], v[148:151], v[184:187], v[208:223]\n\t"
      "s_waitcnt lgkmcnt(0)\n\t"
      "v_mfma_f32_32x32x16_bf16 v[192:207], v[152:155], v[188:191], v[192:207]\n\t"
      "v_mfma_f32_32x32x16_bf16 v[208:223], v[156:159], v[188:191], v[208:223]\n\t"
      "s_mov_b64 exec, 1\n\t"
      "ds_add_u32 v99, v127 offset:28\n\t"
      "s_mov_b64 exec, -1\n\t"
      "Law10_%=:\n\t"
      "ds_read_b32 v103, v99 offset:40\n\t"
      "s_waitcnt lgkmcnt(0)\n\t"
      "v_cmp_ne_u32 vcc, 4, v103\n\t"
      "s_cbranch_vccnz Law10_%=\n\t"
      "s_lshl_b32 s78, s77, 10\n\t"
      "v_add_u32 v111, s78, v58\n\t"
      "v_cmp_eq_u32 s[82:83], 1, v87\n\t"
      "v_pk_mul_f32 v[116:117], v[116:117], s[80:81]\n\t"
      "v_pk_mul_f32 v[118:119], v[118:119], s[80:81]\n\t"
      "v_cvt_pk_bf16_f32 v116, v116, v117\n\t"
      "v_cvt_pk_bf16_f32 v117, v118, v119\n\t"
      "v_pk_mul_f32 v[192:193], v[192:193], s[80:81]\n\t"
      "v_pk_mul_f32 v[194:195], v[194:195], s[80:81]\n\t"
      "v_pk_mul_f32 v[196:197], v[196:197], s[80:81]\n\t"
      "v_pk_mul_f32 v[198:199], v[198:199], s[80:81]\n\t"
      "v_cvt_pk_bf16_f32 v128, v192, v193\n\t"
      "v_cvt_pk_bf16_f32 v129, v194, v195\n\t"
      "v_cvt_pk_bf16_f32 v130, v196, v197\n\t"
      "v_cvt_pk_bf16_f32 v131, v198, v199\n\t"
      "v_cndmask_b32 v129, v129, 0, s[82:83]\n\t"
      "v_cndmask_b32 v130, v130, v116, s[82:83]\n\t"
      "v_cndmask_b32 v131, v131, v117, s[82:83]\n\t"
      "ds_write_b128 v111, v[128:131] offset:43264\n\t"
      "v_pk_mul_f32 v[200:201], v[200:201], s[80:81]\n\t"
      "v_pk_mul_f32 v[202:203], v[202:203], s[80:81]\n\t"
      "v_pk_mul_f32 v[204:205], v[204:205], s[80:81]\n\t"
      "v_pk_mul_f32 v[206:207], v[206:207], s[80:81]\n\t"
      "v_cvt_pk_bf16_f32 v132, v200, v201\n\t"
      "v_cvt_pk_bf16_f32 v133, v202, v203\n\t"
      "v_cvt_pk_bf16_f32 v134, v204, v205\n\t"
      "v_cvt_pk_bf16_f32 v135, v206, v207\n\t"
      "v_cndmask_b32 v133, v133, 0, s[82:83]\n\t"
      "v_cndmask_b32 v134, v134, v116, s[82:83]\n\t"
      "v_cndmask_b32 v135, v135, v117, s[82:83]\n\t"
      "ds_write_b128 v111, v[132:135] offset:47360\n\t"
      "v_pk_mul_f32 v[208:209], v[208:209], s[80:81]\n\t"
      "v_pk_mul_f32 v[210:211], v[210:211], s[80:81]\n\t"
      "v_pk_mul_f32 v[212:213], v[212:213], s[80:81]\n\t"
      "v_pk_mul_f32 v[214:215], v[214:215], s[80:81]\n\t"
      "v_cvt_pk_bf16_f32 v136, v208, v209\n\t"
      "v_cvt_pk_bf16_f32 v137, v210, v211\n\t"
      "v_cvt_pk_bf16_f32 v138, v212, v213\n\t"
      "v_cvt_pk_bf16_f32 v139, v214, v215\n\t"
      "v_cndmask_b32 v137, v137, 0, s[82:83]\n\t"
      "v_cndmask_b32 v138, v138, v116, s[82:83]\n\t"
      "v_cndmask_b32 v139, v139, v117, s[82:83]\n\t"
      "ds_write_b128 v111, v[136:139] offset:51456\n\t"
      "v_pk_mul_f32 v[216:217], v[216:217], s[80:81]\n\t"
      "v_pk_mul_f32 v[218:219], v[218:219], s[80:81]\n\t"
      "v_pk_mul_f32 v[220:221], v[220:221], s[80:81]\n\t"
      "v_pk_mul_f32 v[222:223], v[222:223], s[80:81]\n\t"
      "v_cvt_pk_bf16_f32 v140, v216, v217\n\t"
      "v_cvt_pk_bf16_f32 v141, v218, v219\n\t"
      "v_cvt_pk_bf16_f32 v142, v220, v221\n\t"
      "v_cvt_pk_bf16_f32 v143, v222, v223\n\t"
      "v_cndmask_b32 v141, v141, 0, s[82:83]\n\t"
      "v_cndmask_b32 v142, v142, v116, s[82:83]\n\t"
      "v_cndmask_b32 v143, v143, v117, s[82:83]\n\t"
      "ds_write_b128 v111, v[140:143] offset:55552\n\t"
      "Law11_%=:\n\t"
      "ds_read_b32 v103, v99 offset:28\n\t"
      "s_waitcnt lgkmcnt(0)\n\t"
      "v_cmp_ne_u32 vcc, 4, v103\n\t"
      "s_cbranch_vccnz Law11_%=\n\t"
      "v_bfe_u32 v88, v0, 2, 2\n\t"
      "v_lshl_or_b32 v89, v92, 3, v88\n\t"
      "v_lshlrev_b32 v88, 2, v88\n\t"
      "v_and_b32 v90, 1, v92\n\t"
      "v_lshl_or_b32 v88, v90, 1, v88\n\t"
      "v_bfe_u32 v90, v0, 1, 1\n\t"
      "s_lshl_b32 s78, s48, 1\n\t"
      "v_or_b32 v90, s78, v90\n\t"
      "v_xor_b32 v88, v88, v90\n\t"
      "v_lshlrev_b32 v88, 4, v88\n\t"
      "v_lshl_or_b32 v88, v89, 8, v88\n\t"
      "v_and_b32 v90, 1, v0\n\t"
      "v_lshl_or_b32 v88, v90, 3, v88\n\t"
      "v_add_u32 v236, 59648, v88\n\t"
      "v_xor_b32 v88, 16, v88\n\t"
      "v_add_u32 v237, 60672, v88\n\t"
      "v_bfe_u32 v88, v0, 1, 5\n\t"
      "v_lshlrev_b32 v90, 4, v90\n\t"
      "v_lshl_or_b32 v238, v88, 8, v90\n\t"
      "v_and_b32 v88, 3, v0\n\t"
      "v_lshlrev_b32 v88, 3, v88\n\t"
      "v_lshl_or_b32 v88, v89, 5, v88\n\t"
      "v_add_u32 v239, s42, v88\n\t"
      "s_lshl_b32 s78, s48, 5\n\t"
      "s_add_u32 s78, s78, 32768\n\t"
      "s_add_u32 s40, s22, s78\n\t"
      "s_addc_u32 s41, s23, 0\n\t"
      "s_add_u32 s36, s24, 1024\n\t"
      "s_addc_u32 s37, s25, 0\n\t"
      "s_or_b32 s37, s37, 0x1000000\n\t"
      "s_mov_b32 s38, 10000\n\t"
      "s_mov_b32 s39, 0x20000\n\t"
      "s_mov_b32 s88, s26\n\t"
      "s_mov_b32 s89, s27\n\t"
      "s_mov_b32 s90, 2560000\n\t"
      "s_mov_b32 s91, 0x20000\n\t"
      "v_add_u32 v213, 0xc00, v95\n\t"
      "v_xor_b32 v219, 64, v95\n\t"
      "v_add_u32 v217, 0x400, v95\n\t"
      "v_add_u32 v215, 0x800, v219\n\t"
      "v_mov_b64 v[204:205], 0\n\t"
      "v_mov_b64 v[206:207], 0\n\t"
      "v_mov_b64 v[208:209], 0\n\t"
      "v_mov_b64 v[210:211], 0\n\t"
      "s_nop 0\n\t"
      "v_mfma_f32_32x32x16_bf16 v[2:17], v[204:207], v[204:207], 0\n\t"
      "v_mfma_f32_32x32x16_bf16 v[18:33], v[204:207], v[204:207], 0\n\t"
      "Law12_%=:\n\t"
      "ds_read_b32 v103, v99 offset:52\n\t"
      "s_waitcnt lgkmcnt(0)\n\t"
      "v_cmp_ne_u32 vcc, 4, v103\n\t"
      "s_cbranch_vccnz Law12_%=\n\t"
      "s_movk_i32 s99, 400\n\t"
      "Lplf_%=:\n\t"
      "global_load_dword v83, v126, s[84:85] sc1\n\t"
      "s_waitcnt vmcnt(0)\n\t"
      "v_cmp_eq_u32 vcc, v83, v98\n\t"
      "s_mov_b64 exec, vcc\n\t"
      "s_cbranch_execz Lpdf_%=\n\t"
      "s_sub_u32 s99, s99, 1\n\t"
      "s_sleep 1\n\t"
      "s_cmp_lg_u32 s99, 0\n\t"
      "s_cbranch_scc1 Lplf_%=\n\t"
      "v_mov_b32 v88, 1\n\t"
      "ds_write_b32 v99, v88 offset:16\n\t"
      "s_mov_b32 s98, 1\n\t"
      "Lpdf_%=:\n\t"
      "s_mov_b64 exec, -1\n\t"
      "s_lshl_b32 s79, s77, 13\n\t"
      "s_add_u32 s75, s79, 0\n\t"
      "s_add_u32 s70, s22, s75\n\t"
      "s_addc_u32 s71, s23, 0\n\t"
      "s_add_u32 s72, s70, 4096\n\t"
      "s_addc_u32 s73, s71, 0\n\t"
      "s_add_u32 m0, s79, 59648\n\t"
      "s_nop 0\n\t"
      "global_load_lds_dwordx4 v252, s[70:71] offset:0\n\t"
      "global_load_lds_dwordx4 v253, s[70:71] offset:1024\n\t"
      "global_load_lds_dwordx4 v254, s[70:71] offset:2048\n\t"
      "global_load_lds_dwordx4 v255, s[70:71] offset:3072\n\t"
      "s_add_u32 m0, s79, 63744\n\t"
      "s_nop 0\n\t"
      "global_load_lds_dwordx4 v252, s[72:73] offset:0\n\t"
      "global_load_lds_dwordx4 v253, s[72:73] offset:1024\n\t"
      "global_load_lds_dwordx4 v254, s[72:73] offset:2048\n\t"
      "global_load_lds_dwordx4 v255, s[72:73] offset:3072\n\t"
      "s_branch Ljoin2_%=\n\t"
      "Ljoin_%=:\n\t"
      "s_waitcnt vmcnt(0)\n\t"
      "Ljoin2_%=:\n\t"
      "s_mov_b64 exec, -1\n\t"
      "s_setprio 0\n\t"
      "v_mov_b64 v[176:177], 0\n\t"
      "v_mov_b64 v[178:179], 0\n\t"
      "v_mov_b64 v[180:181], 0\n\t"
      "v_mov_b64 v[182:183], 0\n\t"
      "s_lshr_b32 s44, s48, 1\n\t"
      "s_lshl_b32 s44, s44, 4\n\t"
      "s_waitcnt lgkmcnt(0)\n\t"
      "s_barrier\n\t"
      "ds_read_b96 v[68:70], v99 offset:16\n\t"
      "s_waitcnt lgkmcnt(0)\n\t"
      "v_readfirstlane_b32 s98, v68\n\t"
      "v_readfirstlane_b32 s58, v69\n\t"
      "v_readfirstlane_b32 s59, v70\n\t"
      "s_cmp_lg_u32 s98, 0\n\t"
      "s_cbranch_scc1 Lslow_%=\n\t"
      "s_lshr_b64 s[70:71], s[58:59], s44\n\t"
      "s_and_b32 s94, s70, 3\n\t"
      "s_bfe_u32 s78, s70, 0x60002\n\t"
      "s_mul_i32 s49, s94, 7168\n\t"
      "v_add_u32 v57, s49, v57\n\t"
      "ds_read_u16 v212, v57 offset:0\n\t"
      "ds_read_u16 v214, v57 offset:256\n\t"
      "ds_read_u16 v216, v57 offset:512\n\t"
      "ds_read_u16 v218, v57 offset:768\n\t"
      "s_lshr_b32 s45, s78, 2\n\t"
      "s_and_b32 s46, s78, 3\n\t"
      "s_add_u32 s78, s78, 3\n\t"
      "s_lshr_b32 s43, s78, 2\n\t"
      "s_mov_b32 m0, s42\n\t"
      "s_cmp_lt_i32 s43, 1\n\t"
      "s_waitcnt lgkmcnt(0)\n\t"
      "s_cbranch_scc1 La_%=\n\t"
      "buffer_load_dwordx4 v[212:213], s[36:39], 0 idxen offen offset:0 sc1 lds\n\t"
      "buffer_load_dwordx4 v[214:215], s[36:39], 0 idxen offen offset:1024 sc1 lds\n\t"
      "buffer_load_dwordx4 v[216:217], s[36:39], 0 idxen offen offset:2048 sc1 lds\n\t"
      "buffer_load_dwordx4 v[218:219], s[36:39], 0 idxen offen offset:3072 sc1 lds\n\t"
      "La_%=:\n\t"
      "s_cmp_lg_u32 s48, 3\n\t"
      "s_cbranch_scc1 Lre_%=\n\t"
      "s_cmp_ge_u32 %[r], 128\n\t"
      "s_cbranch_scc1 Lrf_%=\n\t"
      "v_add_u32 v241, 0x10000, v241\n\t"
      "v_add_u32 v243, 0x10000, v243\n\t"
      "v_add_u32 v245, 0x10000, v245\n\t"
      "v_add_u32 v247, 0x10000, v247\n\t"
      "global_store_dwordx4 v58, v[240:243], s[86:87] offset:0 sc1\n\t"
      "global_store_dwordx4 v58, v[244:247], s[86:87] offset:1024 sc1\n\t"
      "s_branch Lre_%=\n\t"
      "Lrf_%=:\n\t"
      "v_add_u32 v125, 2, v125\n\t"
      "s_mov_b64 exec, 1\n\t"
      "global_store_dword v126, v125, s[86:87] sc1\n\t"
      "s_mov_b64 exec, -1\n\t"
      "Lre_%=:\n\t"
      "ds_read_u16 v212, v57 offset:1024\n\t"
      "ds_read_u16 v214, v57 offset:1280\n\t"
      "ds_read_u16 v216, v57 offset:1536\n\t"
      "ds_read_u16 v218, v57 offset:1792\n\t"
      "s_lshl_b32 s78, s94, 12\n\t"
      "s_lshl_b32 s79, s95, 4\n\t"
      "s_add_u32 s78, s78, s79\n\t"
      "v_add_u32 v124, s78, v58\n\t"
      "ds_read_b128 v[34:37], v124 offset:43264\n\t"
      "ds_read_b128 v[38:41], v124 offset:44288\n\t"
      "v_add_u32 v54, s49, v54\n\t"
      "v_add_u32 v55, s49, v55\n\t"
      "s_mul_i32 s78, s94, 2720\n\t"
      "v_add_u32 v56, 0x100, v55\n\t"
      "v_add_u32 v59, s78, v59\n\t"
      "s_cmp_lt_u32 s48, 4\n\t"
      "s_cbranch_scc1 Lzp_%=\n\t"
      "s_cmp_lt_i32 s43, 1\n\t"
      "s_cbranch_scc1 Lzq_%=\n\t"
      "s_waitcnt vmcnt(4)\n\t"
      "s_branch Lzp_%=\n\t"
      "Lzq_%=:\n\t"
      "s_waitcnt vmcnt(0)\n\t"
      "Lzp_%=:\n\t"
      "s_waitcnt lgkmcnt(0)\n\t"
      "s_barrier\n\t"
      "ds_read_b64_tr_b16 v[184:185], v236 offset:0\n\t"
      "ds_read_b64_tr_b16 v[186:187], v237 offset:0\n\t"
      "ds_read_b128 v[128:131], v60 offset:33024\n\t"
      "ds_read_b128 v[132:135], v60 offset:37120\n\t"
      "ds_read_b128 v[136:139], v60 offset:41216\n\t"
      "ds_read_b64_tr_b16 v[188:189], v236 offset:8192\n\t"
      "ds_read_b64_tr_b16 v[190:191], v237 offset:8192\n\t"
      "ds_read_b128 v[140:143], v61 offset:33024\n\t"
      "ds_read_b128 v[144:147], v61 offset:37120\n\t"
      "ds_read_b128 v[148:151], v61 offset:41216\n\t"
      "ds_read_b64_tr_b16 v[192:193], v236 offset:16384\n\t"
      "ds_read_b64_tr_b16 v[194:195], v237 offset:16384\n\t"
      "ds_read_b128 v[152:155], v62 offset:33024\n\t"
      "ds_read_b128 v[156:159], v62 offset:37120\n\t"
      "ds_read_b128 v[160:163], v62 offset:41216\n\t"
      "s_waitcnt lgkmcnt(10)\n\t"
      "v_mfma_f32_16x16x32_bf16 v[224:227], v[184:187], v[128:131], 0\n\t"
      "v_mfma_f32_16x16x32_bf16 v[228:231], v[184:187], v[132:135], 0\n\t"
      "v_mfma_f32_16x16x32_bf16 v[232:235], v[184:187], v[136:139], 0\n\t"
      "ds_read_b64_tr_b16 v[196:197], v236 offset:24576\n\t"
      "ds_read_b64_tr_b16 v[198:199], v237 offset:24576\n\t"
      "ds_read_b128 v[164:167], v63 offset:33024\n\t"
      "ds_read_b128 v[168:171], v63 offset:37120\n\t"
      "ds_read_b128 v[172:175], v63 offset:41216\n\t"
      "s_waitcnt lgkmcnt(10)\n\t"
      "v_mfma_f32_16x16x32_bf16 v[224:227], v[188:191], v[140:143], v[224:227]\n\t"
      "v_mfma_f32_16x16x32_bf16 v[228:231], v[188:191], v[144:147], v[228:231]\n\t"
      "v_mfma_f32_16x16x32_bf16 v[232:235], v[188:191], v[148:151], v[232:235]\n\t"
      "s_waitcnt lgkmcnt(5)\n\t"
      "v_mfma_f32_16x16x32_bf16 v[224:227], v[192:195], v[152:155], v[224:227]\n\t"
      "v_mfma_f32_16x16x32_bf16 v[228:231], v[192:195], v[156:159], v[228:231]\n\t"
      "v_mfma_f32_16x16x32_bf16 v[232:235], v[192:195], v[160:163], v[232:235]\n\t"
      "s_waitcnt lgkmcnt(0)\n\t"
      "v_mfma_f32_16x16x32_bf16 v[224:227], v[196:199], v[164:167], v[224:227]\n\t"
      "v_mfma_f32_16x16x32_bf16 v[228:231], v[196:199], v[168:171], v[228:231]\n\t"
      "v_mfma_f32_16x16x32_bf16 v[232:235], v[196:199], v[172:175], v[232:235]\n\t"
      "s_barrier\n\t"
      "s_and_b32 s78, s43, 1\n\t"
      "s_lshl_b32 s78, s78, 12\n\t"
      "v_add_u32 v239, s78, v239\n\t"
      "s_nop 1\n\t"
      "v_pk_mul_f32 v[224:225], v[224:225], s[80:81]\n\t"
      "v_pk_mul_f32 v[226:227], v[226:227], s[80:81]\n\t"
      "v_pk_mul_f32 v[228:229], v[228:229], s[80:81]\n\t"
      "v_pk_mul_f32 v[230:231], v[230:231], s[80:81]\n\t"
      "v_pk_mul_f32 v[232:233], v[232:233], s[80:81]\n\t"
      "v_pk_mul_f32 v[234:235], v[234:235], s[80:81]\n\t"
      "s_sub_i32 s43, s43, 1\n\t"
      "s_cmp_lt_i32 s43, 0\n\t"
      "s_cbranch_scc1 Lnone_%=\n\t"
      "ds_read_u16 v160, v57 offset:2048\n\t"
      "ds_read_u16 v162, v57 offset:2304\n\t"
      "ds_read_u16 v164, v57 offset:2560\n\t"
      "ds_read_u16 v166, v57 offset:2816\n\t"
      "ds_read_b128 v[200:203], v54 offset:0\n\t"
      "v_mov_b32 v161, v213\n\t"
      "v_mov_b32 v163, v215\n\t"
      "v_mov_b32 v165, v217\n\t"
      "v_mov_b32 v167, v219\n\t"
      "s_setprio 1\n\t"
      "s_cmp_gt_u32 s48, 3\n\t"
      "s_cbranch_scc1 Lyg_%=\n\t"
      "s_add_u32 m0, s42, 4096\n\t"
      "s_cmp_lt_i32 s43, 1\n\t"
      "s_waitcnt lgkmcnt(0)\n\t"
      "s_cbranch_scc1 Lnd13_%=\n\t"
      "buffer_load_dwordx4 v[212:213], s[36:39], 0 idxen offen offset:0 sc1 lds\n\t"
      "buffer_load_dwordx4 v[214:215], s[36:39], 0 idxen offen offset:1024 sc1 lds\n\t"
      "buffer_load_dwordx4 v[216:217], s[36:39], 0 idxen offen offset:2048 sc1 lds\n\t"
      "buffer_load_dwordx4 v[218:219], s[36:39], 0 idxen offen offset:3072 sc1 lds\n\t"
      "Lge13_%=:\n\t"
      "s_sub_i32 s43, s43, 1\n\t"
      "s_waitcnt vmcnt(4)\n\t"
      "ds_read_b128 v[184:187], v50 offset:0\n\t"
      "ds_read_b128 v[188:191], v51 offset:0\n\t"
      "ds_read_b128 v[192:195], v52 offset:0\n\t"
      "ds_read_b128 v[196:199], v53 offset:0\n\t"
      "v_mfma_f32_32x32x16_bf16 v[128:143], v[200:203], v[34:37], 0\n\t"
      "s_branch Lg2_%=\n\t"
      "Lyg_%=:\n\t"
      "s_waitcnt vmcnt(0) lgkmcnt(0)\n\t"
      "ds_read_b128 v[184:187], v50 offset:0\n\t"
      "ds_read_b128 v[188:191], v51 offset:0\n\t"
      "ds_read_b128 v[192:195], v52 offset:0\n\t"
      "ds_read_b128 v[196:199], v53 offset:0\n\t"
      "v_mfma_f32_32x32x16_bf16 v[128:143], v[200:203], v[34:37], 0\n\t"
      "s_add_u32 m0, s42, 4096\n\t"
      "s_cmp_lt_i32 s43, 1\n\t"
      "s_nop 0\n\t"
      "s_cbranch_scc1 Lnd14_%=\n\t"
      "buffer_load_dwordx4 v[212:213], s[36:39], 0 idxen offen offset:0 sc1 lds\n\t"
      "buffer_load_dwordx4 v[214:215], s[36:39], 0 idxen offen offset:1024 sc1 lds\n\t"
      "buffer_load_dwordx4 v[216:217], s[36:39], 0 idxen offen offset:2048 sc1 lds\n\t"
      "buffer_load_dwordx4 v[218:219], s[36:39], 0 idxen offen offset:3072 sc1 lds\n\t"
      "Lge14_%=:\n\t"
      "s_sub_i32 s43, s43, 1\n\t"
      "Lg2_%=:\n\t"
      "s_waitcnt lgkmcnt(0)\n\t"
      "v_mfma_f32_32x32x16_bf16 v[128:143], v[184:187], v[42:45], v[128:143]\n\t"
      "v_mfma_f32_32x32x16_bf16 v[128:143], v[188:191], v[46:49], v[128:143]\n\t"
      "s_add_u32 m0, s42, 0\n\t"
      "s_cmp_lt_i32 s43, 1\n\t"
      "s_nop 0\n\t"
      "s_cbranch_scc1 Lnd15_%=\n\t"
      "buffer_load_dwordx4 v[160:161], s[36:39], 0 idxen offen offset:0 sc1 lds\n\t"
      "buffer_load_dwordx4 v[162:163], s[36:39], 0 idxen offen offset:1024 sc1 lds\n\t"
      "buffer_load_dwordx4 v[164:165], s[36:39], 0 idxen offen offset:2048 sc1 lds\n\t"
      "buffer_load_dwordx4 v[166:167], s[36:39], 0 idxen offen offset:3072 sc1 lds\n\t"
      "Lge15_%=:\n\t"
      "s_sub_i32 s43, s43, 1\n\t"
      "s_mov_b32 s44, s45\n\t"
      "s_cmp_eq_u32 s44, 0\n\t"
      "s_nop 3\n\t"
      "s_cbranch_scc1 Ltail_%=\n\t"
      "Lloop_%=:\n\t"
      "v_exp_f32 v160, v128\n\t"
      "v_exp_f32 v161, v129\n\t"
      "v_mfma_f32_32x32x16_bf16 v[18:33], v[176:179], v[204:207], v[18:33]\n\t"
      "v_exp_f32 v162, v130\n\t"
      "v_exp_f32 v163, v131\n\t"
      "v_exp_f32 v164, v132\n\t"
      "v_exp_f32 v165, v133\n\t"
      "v_mfma_f32_32x32x16_bf16 v[18:33], v[180:183], v[208:211], v[18:33]\n\t"
      "v_exp_f32 v166, v134\n\t"
      "v_exp_f32 v167, v135\n\t"
      "v_exp_f32 v168, v136\n\t"
      "v_exp_f32 v169, v137\n\t"
      "ds_read_b64_tr_b16 v[204:205], v55 offset:0\n\t"
      "ds_read_b64_tr_b16 v[206:207], v56 offset:0\n\t"
      "ds_read_b64_tr_b16 v[208:209], v55 offset:512\n\t"
      "ds_read_b64_tr_b16 v[210:211], v56 offset:512\n\t"
      "ds_read_u16 v212, v57 offset:3072\n\t"
      "ds_read_u16 v214, v57 offset:3328\n\t"
      "ds_read_u16 v216, v57 offset:3584\n\t"
      "ds_read_u16 v218, v57 offset:3840\n\t"
      "v_exp_f32 v170, v138\n\t"
      "v_exp_f32 v171, v139\n\t"
      "v_exp_f32 v172, v140\n\t"
      "v_exp_f32 v173, v141\n\t"
      "v_exp_f32 v174, v142\n\t"
      "v_exp_f32 v175, v143\n\t"
      "v_pk_add_f32 v[160:161], v[160:161], 1.0 op_sel_hi:[1,0]\n\t"
      "v_pk_add_f32 v[162:163], v[162:163], 1.0 op_sel_hi:[1,0]\n\t"
      "v_pk_add_f32 v[164:165], v[164:165], 1.0 op_sel_hi:[1,0]\n\t"
      "v_pk_add_f32 v[166:167], v[166:167], 1.0 op_sel_hi:[1,0]\n\t"
      "v_pk_add_f32 v[168:169], v[168:169], 1.0 op_sel_hi:[1,0]\n\t"
      "v_pk_add_f32 v[170:171], v[170:171], 1.0 op_sel_hi:[1,0]\n\t"
      "v_pk_add_f32 v[172:173], v[172:173], 1.0 op_sel_hi:[1,0]\n\t"
      "v_pk_add_f32 v[174:175], v[174:175], 1.0 op_sel_hi:[1,0]\n\t"
      "v_mfma_f32_32x32x16_bf16 v[144:159], v[200:203], v[38:41], 0\n\t"
      "v_rcp_f32 v160, v160\n\t"
      "v_rcp_f32 v161, v161\n\t"
      "v_rcp_f32 v162, v162\n\t"
      "v_rcp_f32 v163, v163\n\t"
      "v_mfma_f32_32x32x16_bf16 v[144:159], v[192:195], v[42:45], v[144:159]\n\t"
      "v_rcp_f32 v164, v164\n\t"
      "v_rcp_f32 v165, v165\n\t"
      "v_rcp_f32 v166, v166\n\t"
      "v_rcp_f32 v167, v167\n\t"
      "v_mfma_f32_32x32x16_bf16 v[144:159], v[196:199], v[46:49], v[144:159]\n\t"
      "v_rcp_f32 v168, v168\n\t"
      "v_rcp_f32 v169, v169\n\t"
      "v_rcp_f32 v170, v170\n\t"
      "v_rcp_f32 v171, v171\n\t"
      "v_rcp_f32 v172, v172\n\t"
      "v_rcp_f32 v173, v173\n\t"
      "v_rcp_f32 v174, v174\n\t"
      "v_rcp_f32 v175, v175\n\t"
      "v_pk_mul_f32 v[160:161], v[128:129], v[160:161]\n\t"
      "v_pk_mul_f32 v[162:163], v[130:131], v[162:163]\n\t"
      "v_pk_mul_f32 v[164:165], v[132:133], v[164:165]\n\t"
      "v_pk_mul_f32 v[166:167], v[134:135], v[166:167]\n\t"
      "v_pk_mul_f32 v[168:169], v[136:137], v[168:169]\n\t"
      "v_pk_mul_f32 v[170:171], v[138:139], v[170:171]\n\t"
      "v_pk_mul_f32 v[172:173], v[140:141], v[172:173]\n\t"
      "v_pk_mul_f32 v[174:175], v[142:143], v[174:175]\n\t"
      "v_cvt_pk_bf16_f32 v176, v160, v161\n\t"
      "v_cvt_pk_bf16_f32 v177, v162, v163\n\t"
      "v_cvt_pk_bf16_f32 v178, v164, v165\n\t"
      "v_cvt_pk_bf16_f32 v179, v166, v167\n\t"
      "v_cvt_pk_bf16_f32 v180, v168, v169\n\t"
      "v_cvt_pk_bf16_f32 v181, v170, v171\n\t"
      "v_cvt_pk_bf16_f32 v182, v172, v173\n\t"
      "v_cvt_pk_bf16_f32 v183, v174, v175\n\t"
      "s_waitcnt lgkmcnt(0)\n\t"
      "v_exp_f32 v160, v144\n\t"
      "v_exp_f32 v161, v145\n\t"
      "v_mfma_f32_32x32x16_bf16 v[2:17], v[176:179], v[204:207], v[2:17]\n\t"
      "v_exp_f32 v162, v146\n\t"
      "v_exp_f32 v163, v147\n\t"
      "v_exp_f32 v164, v148\n\t"
      "v_exp_f32 v165, v149\n\t"
      "v_mfma_f32_32x32x16_bf16 v[2:17], v[180:183], v[208:211], v[2:17]\n\t"
      "v_exp_f32 v166, v150\n\t"
      "v_exp_f32 v167, v151\n\t"
      "v_exp_f32 v168, v152\n\t"
      "v_exp_f32 v169, v153\n\t"
      "s_waitcnt vmcnt(4)\n\t"
      "ds_read_b128 v[200:203], v54 offset:1024\n\t"
      "ds_read_b128 v[184:187], v50 offset:4096\n\t"
      "ds_read_b128 v[188:191], v51 offset:4096\n\t"
      "ds_read_b128 v[192:195], v52 offset:4096\n\t"
      "ds_read_b128 v[196:199], v53 offset:4096\n\t"
      "v_exp_f32 v170, v154\n\t"
      "v_exp_f32 v171, v155\n\t"
      "v_exp_f32 v172, v156\n\t"
      "v_exp_f32 v173, v157\n\t"
      "v_exp_f32 v174, v158\n\t"
      "v_exp_f32 v175, v159\n\t"
      "v_pk_add_f32 v[160:161], v[160:161], 1.0 op_sel_hi:[1,0]\n\t"
      "v_pk_add_f32 v[162:163], v[162:163], 1.0 op_sel_hi:[1,0]\n\t"
      "v_pk_add_f32 v[164:165], v[164:165], 1.0 op_sel_hi:[1,0]\n\t"
      "v_pk_add_f32 v[166:167], v[166:167], 1.0 op_sel_hi:[1,0]\n\t"
      "v_pk_add_f32 v[168:169], v[168:169], 1.0 op_sel_hi:[1,0]\n\t"
      "v_pk_add_f32 v[170:171], v[170:171], 1.0 op_sel_hi:[1,0]\n\t"
      "v_pk_add_f32 v[172:173], v[172:173], 1.0 op_sel_hi:[1,0]\n\t"
      "v_pk_add_f32 v[174:175], v[174:175], 1.0 op_sel_hi:[1,0]\n\t"
      "s_waitcnt lgkmcnt(2)\n\t"
      "v_mfma_f32_32x32x16_bf16 v[128:143], v[200:203], v[34:37], 0\n\t"
      "v_rcp_f32 v160, v160\n\t"
      "v_rcp_f32 v161, v161\n\t"
      "v_rcp_f32 v162, v162\n\t"
      "v_rcp_f32 v163, v163\n\t"
      "v_mfma_f32_32x32x16_bf16 v[128:143], v[184:187], v[42:45], v[128:143]\n\t"
      "v_rcp_f32 v164, v164\n\t"
      "v_rcp_f32 v165, v165\n\t"
      "v_rcp_f32 v166, v166\n\t"
      "v_rcp_f32 v167, v167\n\t"
      "v_mfma_f32_32x32x16_bf16 v[128:143], v[188:191], v[46:49], v[128:143]\n\t"
      "v_rcp_f32 v168, v168\n\t"
      "v_rcp_f32 v169, v169\n\t"
      "v_rcp_f32 v170, v170\n\t"
      "v_rcp_f32 v171, v171\n\t"
      "v_rcp_f32 v172, v172\n\t"
      "v_rcp_f32 v173, v173\n\t"
      "v_rcp_f32 v174, v174\n\t"
      "v_rcp_f32 v175, v175\n\t"
      "s_add_u32 m0, s42, 4096\n\t"
      "s_cmp_lt_i32 s43, 1\n\t"
      "s_waitcnt lgkmcnt(0)\n\t"
      "s_cbranch_scc1 Lnd16_%=\n\t"
      "buffer_load_dwordx4 v[212:213], s[36:39], 0 idxen offen offset:0 sc1 lds\n\t"
      "buffer_load_dwordx4 v[214:215], s[36:39], 0 idxen offen offset:1024 sc1 lds\n\t"
      "buffer_load_dwordx4 v[216:217], s[36:39], 0 idxen offen offset:2048 sc1 lds\n\t"
      "buffer_load_dwordx4 v[218:219], s[36:39], 0 idxen offen offset:3072 sc1 lds\n\t"
      "Lge16_%=:\n\t"
      "s_sub_i32 s43, s43, 1\n\t"
      "v_pk_mul_f32 v[160:161], v[144:145], v[160:161]\n\t"
      "v_pk_mul_f32 v[162:163], v[146:147], v[162:163]\n\t"
      "v_pk_mul_f32 v[164:165], v[148:149], v[164:165]\n\t"
      "v_pk_mul_f32 v[166:167], v[150:151], v[166:167]\n\t"
      "v_pk_mul_f32 v[168:169], v[152:153], v[168:169]\n\t"
      "v_pk_mul_f32 v[170:171], v[154:155], v[170:171]\n\t"
      "v_pk_mul_f32 v[172:173], v[156:157], v[172:173]\n\t"
      "v_pk_mul_f32 v[174:175], v[158:159], v[174:175]\n\t"
      "v_cvt_pk_bf16_f32 v176, v160, v161\n\t"
      "v_cvt_pk_bf16_f32 v177, v162, v163\n\t"
      "v_cvt_pk_bf16_f32 v178, v164, v165\n\t"
      "v_cvt_pk_bf16_f32 v179, v166, v167\n\t"
      "v_cvt_pk_bf16_f32 v180, v168, v169\n\t"
      "v_cvt_pk_bf16_f32 v181, v170, v171\n\t"
      "v_cvt_pk_bf16_f32 v182, v172, v173\n\t"
      "v_cvt_pk_bf16_f32 v183, v174, v175\n\t"
      "s_sub_u32 s44, s44, 1\n\t"
      "s_cmp_eq_u32 s44, 0\n\t"
      "s_cbranch_scc1 Lexit_even_%=\n\t"
      "v_exp_f32 v160, v128\n\t"
      "v_exp_f32 v161, v129\n\t"
      "v_mfma_f32_32x32x16_bf16 v[18:33], v[176:179], v[204:207], v[18:33]\n\t"
      "v_exp_f32 v162, v130\n\t"
      "v_exp_f32 v163, v131\n\t"
      "v_exp_f32 v164, v132\n\t"
      "v_exp_f32 v165, v133\n\t"
      "v_mfma_f32_32x32x16_bf16 v[18:33], v[180:183], v[208:211], v[18:33]\n\t"
      "v_exp_f32 v166, v134\n\t"
      "v_exp_f32 v167, v135\n\t"
      "v_exp_f32 v168, v136\n\t"
      "v_exp_f32 v169, v137\n\t"
      "ds_read_b64_tr_b16 v[204:205], v55 offset:1024\n\t"
      "ds_read_b64_tr_b16 v[206:207], v56 offset:1024\n\t"
      "ds_read_b64_tr_b16 v[208:209], v55 offset:1536\n\t"
      "ds_read_b64_tr_b16 v[210:211], v56 offset:1536\n\t"
      "ds_read_u16 v212, v57 offset:4096\n\t"
      "ds_read_u16 v214, v57 offset:4352\n\t"
      "ds_read_u16 v216, v57 offset:4608\n\t"
      "ds_read_u16 v218, v57 offset:4864\n\t"
      "v_exp_f32 v170, v138\n\t"
      "v_exp_f32 v171, v139\n\t"
      "v_exp_f32 v172, v140\n\t"
      "v_exp_f32 v173, v141\n\t"
      "v_exp_f32 v174, v142\n\t"
      "v_exp_f32 v175, v143\n\t"
      "v_pk_add_f32 v[160:161], v[160:161], 1.0 op_sel_hi:[1,0]\n\t"
      "v_pk_add_f32 v[162:163], v[162:163], 1.0 op_sel_hi:[1,0]\n\t"
      "v_pk_add_f32 v[164:165], v[164:165], 1.0 op_sel_hi:[1,0]\n\t"
      "v_pk_add_f32 v[166:167], v[166:167], 1.0 op_sel_hi:[1,0]\n\t"
      "v_pk_add_f32 v[168:169], v[168:169], 1.0 op_sel_hi:[1,0]\n\t"
      "v_pk_add_f32 v[170:171], v[170:171], 1.0 op_sel_hi:[1,0]\n\t"
      "v_pk_add_f32 v[172:173], v[172:173], 1.0 op_sel_hi:[1,0]\n\t"
      "v_pk_add_f32 v[174:175], v[174:175], 1.0 op_sel_hi:[1,0]\n\t"
      "v_mfma_f32_32x32x16_bf16 v[144:159], v[200:203], v[38:41], 0\n\t"
      "v_rcp_f32 v160, v160\n\t"
      "v_rcp_f32 v161, v161\n\t"
      "v_rcp_f32 v162, v162\n\t"
      "v_rcp_f32 v163, v163\n\t"
      "v_mfma_f32_32x32x16_bf16 v[144:159], v[192:195], v[42:45], v[144:159]\n\t"
      "v_rcp_f32 v164, v164\n\t"
      "v_rcp_f32 v165, v165\n\t"
      "v_rcp_f32 v166, v166\n\t"
      "v_rcp_f32 v167, v167\n\t"
      "v_mfma_f32_32x32x16_bf16 v[144:159], v[196:199], v[46:49], v[144:159]\n\t"
      "v_rcp_f32 v168, v168\n\t"
      "v_rcp_f32 v169, v169\n\t"
      "v_rcp_f32 v170, v170\n\t"
      "v_rcp_f32 v171, v171\n\t"
      "v_rcp_f32 v172, v172\n\t"
      "v_rcp_f32 v173, v173\n\t"
      "v_rcp_f32 v174, v174\n\t"
      "v_rcp_f32 v175, v175\n\t"
      "v_pk_mul_f32 v[160:161], v[128:129], v[160:161]\n\t"
      "v_pk_mul_f32 v[162:163], v[130:131], v[162:163]\n\t"
      "v_pk_mul_f32 v[164:165], v[132:133], v[164:165]\n\t"
      "v_pk_mul_f32 v[166:167], v[134:135], v[166:167]\n\t"
      "v_pk_mul_f32 v[168:169], v[136:137], v[168:169]\n\t"
      "v_pk_mul_f32 v[170:171], v[138:139], v[170:171]\n\t"
      "v_pk_mul_f32 v[172:173], v[140:141], v[172:173]\n\t"
      "v_pk_mul_f32 v[174:175], v[142:143], v[174:175]\n\t"
      "v_cvt_pk_bf16_f32 v176, v160, v161\n\t"
      "v_cvt_pk_bf16_f32 v177, v162, v163\n\t"
      "v_cvt_pk_bf16_f32 v178, v164, v165\n\t"
      "v_cvt_pk_bf16_f32 v179, v166, v167\n\t"
      "v_cvt_pk_bf16_f32 v180, v168, v169\n\t"
      "v_cvt_pk_bf16_f32 v181, v170, v171\n\t"
      "v_cvt_pk_bf16_f32 v182, v172, v173\n\t"
      "v_cvt_pk_bf16_f32 v183, v174, v175\n\t"
      "s_waitcnt lgkmcnt(0)\n\t"
      "v_exp_f32 v160, v144\n\t"
      "v_exp_f32 v161, v145\n\t"
      "v_mfma_f32_32x32x16_bf16 v[2:17], v[176:179], v[204:207], v[2:17]\n\t"
      "v_exp_f32 v162, v146\n\t"
      "v_exp_f32 v163, v147\n\t"
      "v_exp_f32 v164, v148\n\t"
      "v_exp_f32 v165, v149\n\t"
      "v_mfma_f32_32x32x16_bf16 v[2:17], v[180:183], v[208:211], v[2:17]\n\t"
      "v_exp_f32 v166, v150\n\t"
      "v_exp_f32 v167, v151\n\t"
      "v_exp_f32 v168, v152\n\t"
      "v_exp_f32 v169, v153\n\t"
      "s_waitcnt vmcnt(4)\n\t"
      "ds_read_b128 v[200:203], v54 offset:2048\n\t"
      "ds_read_b128 v[184:187], v50 offset:0\n\t"
      "ds_read_b128 v[188:191], v51 offset:0\n\t"
      "ds_read_b128 v[192:195], v52 offset:0\n\t"
      "ds_read_b128 v[196:199], v53 offset:0\n\t"
      "v_exp_f32 v170, v154\n\t"
      "v_exp_f32 v171, v155\n\t"
      "v_exp_f32 v172, v156\n\t"
      "v_exp_f32 v173, v157\n\t"
      "v_exp_f32 v174, v158\n\t"
      "v_exp_f32 v175, v159\n\t"
      "v_pk_add_f32 v[160:161], v[160:161], 1.0 op_sel_hi:[1,0]\n\t"
      "v_pk_add_f32 v[162:163], v[162:163], 1.0 op_sel_hi:[1,0]\n\t"
      "v_pk_add_f32 v[164:165], v[164:165], 1.0 op_sel_hi:[1,0]\n\t"
      "v_pk_add_f32 v[166:167], v[166:167], 1.0 op_sel_hi:[1,0]\n\t"
      "v_pk_add_f32 v[168:169], v[168:169], 1.0 op_sel_hi:[1,0]\n\t"
      "v_pk_add_f32 v[170:171], v[170:171], 1.0 op_sel_hi:[1,0]\n\t"
      "v_pk_add_f32 v[172:173], v[172:173], 1.0 op_sel_hi:[1,0]\n\t"
      "v_pk_add_f32 v[174:175], v[174:175], 1.0 op_sel_hi:[1,0]\n\t"
      "s_waitcnt lgkmcnt(2)\n\t"
      "v_mfma_f32_32x32x16_bf16 v[128:143], v[200:203], v[34:37], 0\n\t"
      "v_rcp_f32 v160, v160\n\t"
      "v_rcp_f32 v161, v161\n\t"
      "v_rcp_f32 v162, v162\n\t"
      "v_rcp_f32 v163, v163\n\t"
      "v_mfma_f32_32x32x16_bf16 v[128:143], v[184:187], v[42:45], v[128:143]\n\t"
      "v_rcp_f32 v164, v164\n\t"
      "v_rcp_f32 v165, v165\n\t"
      "v_rcp_f32 v166, v166\n\t"
      "v_rcp_f32 v167, v167\n\t"
      "v_mfma_f32_32x32x16_bf16 v[128:143], v[188:191], v[46:49], v[128:143]\n\t"
      "v_rcp_f32 v168, v168\n\t"
      "v_rcp_f32 v169, v169\n\t"
      "v_rcp_f32 v170, v170\n\t"
      "v_rcp_f32 v171, v171\n\t"
      "v_rcp_f32 v172, v172\n\t"
      "v_rcp_f32 v173, v173\n\t"
      "v_rcp_f32 v174, v174\n\t"
      "v_rcp_f32 v175, v175\n\t"
      "s_add_u32 m0, s42, 0\n\t"
      "s_cmp_lt_i32 s43, 1\n\t"
      "s_waitcnt lgkmcnt(0)\n\t"
      "s_cbranch_scc1 Lnd17_%=\n\t"
      "buffer_load_dwordx4 v[212:213], s[36:39], 0 idxen offen offset:0 sc1 lds\n\t"
      "buffer_load_dwordx4 v[214:215], s[36:39], 0 idxen offen offset:1024 sc1 lds\n\t"
      "buffer_load_dwordx4 v[216:217], s[36:39], 0 idxen offen offset:2048 sc1 lds\n\t"
      "buffer_load_dwordx4 v[218:219], s[36:39], 0 idxen offen offset:3072 sc1 lds\n\t"
      "Lge17_%=:\n\t"
      "s_sub_i32 s43, s43, 1\n\t"
      "v_pk_mul_f32 v[160:161], v[144:145], v[160:161]\n\t"
      "v_pk_mul_f32 v[162:163], v[146:147], v[162:163]\n\t"
      "v_pk_mul_f32 v[164:165], v[148:149], v[164:165]\n\t"
      "v_pk_mul_f32 v[166:167], v[150:151], v[166:167]\n\t"
      "v_pk_mul_f32 v[168:169], v[152:153], v[168:169]\n\t"
      "v_pk_mul_f32 v[170:171], v[154:155], v[170:171]\n\t"
      "v_pk_mul_f32 v[172:173], v[156:157], v[172:173]\n\t"
      "v_pk_mul_f32 v[174:175], v[158:159], v[174:175]\n\t"
      "v_cvt_pk_bf16_f32 v176, v160, v161\n\t"
      "v_cvt_pk_bf16_f32 v177, v162, v163\n\t"
      "v_cvt_pk_bf16_f32 v178, v164, v165\n\t"
      "v_cvt_pk_bf16_f32 v179, v166, v167\n\t"
      "v_cvt_pk_bf16_f32 v180, v168, v169\n\t"
      "v_cvt_pk_bf16_f32 v181, v170, v171\n\t"
      "v_cvt_pk_bf16_f32 v182, v172, v173\n\t"
      "v_cvt_pk_bf16_f32 v183, v174, v175\n\t"
      "v_add_u32 v54, 0x800, v54\n\t"
      "v_add_u32 v55, 0x800, v55\n\t"
      "v_add_u32 v56, 0x800, v56\n\t"
      "v_add_u32 v57, 0x800, v57\n\t"
      "s_sub_u32 s44, s44, 1\n\t"
      "s_cmp_lg_u32 s44, 0\n\t"
      "s_cbranch_scc1 Lloop_%=\n\t"
      "s_branch Ltail_%=\n\t"
      "Lnd13_%=:\n\t"
      "s_cmp_eq_i32 s43, 0\n\t"
      "s_cbranch_scc1 Lnw13_%=\n\t"
      "s_setprio 0\n\t"
      "s_branch Lge13_%=\n\t"
      "Lnw13_%=:\n\t"
      "global_load_lds_dwordx4 v238, s[40:41] offset:0\n\t"
      "s_add_u32 s40, s40, 7168\n\t"
      "s_addc_u32 s41, s41, 0\n\t"
      "global_load_lds_dwordx4 v238, s[40:41] offset:1024\n\t"
      "s_add_u32 s40, s40, 7168\n\t"
      "s_addc_u32 s41, s41, 0\n\t"
      "global_load_lds_dwordx4 v238, s[40:41] offset:2048\n\t"
      "s_add_u32 s40, s40, 7168\n\t"
      "s_addc_u32 s41, s41, 0\n\t"
      "global_load_lds_dwordx4 v238, s[40:41] offset:3072\n\t"
      "s_branch Lge13_%=\n\t"
      "Lnd14_%=:\n\t"
      "s_cmp_eq_i32 s43, 0\n\t"
      "s_cbranch_scc1 Lnw14_%=\n\t"
      "s_setprio 0\n\t"
      "s_branch Lge14_%=\n\t"
      "Lnw14_%=:\n\t"
      "global_load_lds_dwordx4 v238, s[40:41] offset:0\n\t"
      "s_add_u32 s40, s40, 7168\n\t"
      "s_addc_u32 s41, s41, 0\n\t"
      "global_load_lds_dwordx4 v238, s[40:41] offset:1024\n\t"
      "s_add_u32 s40, s40, 7168\n\t"
      "s_addc_u32 s41, s41, 0\n\t"
      "global_load_lds_dwordx4 v238, s[40:41] offset:2048\n\t"
      "s_add_u32 s40, s40, 7168\n\t"
      "s_addc_u32 s41, s41, 0\n\t"
      "global_load_lds_dwordx4 v238, s[40:41] offset:3072\n\t"
      "s_branch Lge14_%=\n\t"
      "Lnd15_%=:\n\t"
      "s_cmp_eq_i32 s43, 0\n\t"
      "s_cbranch_scc1 Lnw15_%=\n\t"
      "s_setprio 0\n\t"
      "s_branch Lge15_%=\n\t"
      "Lnw15_%=:\n\t"
      "global_load_lds_dwordx4 v238, s[40:41] offset:0\n\t"
      "s_add_u32 s40, s40, 7168\n\t"
      "s_addc_u32 s41, s41, 0\n\t"
      "global_load_lds_dwordx4 v238, s[40:41] offset:1024\n\t"
      "s_add_u32 s40, s40, 7168\n\t"
      "s_addc_u32 s41, s41, 0\n\t"
      "global_load_lds_dwordx4 v238, s[40:41] offset:2048\n\t"
      "s_add_u32 s40, s40, 7168\n\t"
      "s_addc_u32 s41, s41, 0\n\t"
      "global_load_lds_dwordx4 v238, s[40:41] offset:3072\n\t"
      "s_branch Lge15_%=\n\t"
      "Lnone_%=:\n\t"
      "s_mov_b32 m0, s42\n\t"
      "s_nop 0\n\t"
      "global_load_lds_dwordx4 v238, s[40:41] offset:0\n\t"
      "s_add_u32 s40, s40, 7168\n\t"
      "s_addc_u32 s41, s41, 0\n\t"
      "global_load_lds_dwordx4 v238, s[40:41] offset:1024\n\t"
      "s_add_u32 s40, s40, 7168\n\t"
      "s_addc_u32 s41, s41, 0\n\t"
      "global_load_lds_dwordx4 v238, s[40:41] offset:2048\n\t"
      "s_add_u32 s40, s40, 7168\n\t"
      "s_addc_u32 s41, s41, 0\n\t"
      "global_load_lds_dwordx4 v238, s[40:41] offset:3072\n\t"
      "s_branch Ldone_%=\n\t"
      "Lnd16_%=:\n\t"
      "s_cmp_eq_i32 s43, 0\n\t"
      "s_cbranch_scc1 Lnw16_%=\n\t"
      "s_setprio 0\n\t"
      "s_branch Lge16_%=\n\t"
      "Lnw16_%=:\n\t"
      "global_load_lds_dwordx4 v238, s[40:41] offset:0\n\t"
      "s_add_u32 s40, s40, 7168\n\t"
      "s_addc_u32 s41, s41, 0\n\t"
      "global_load_lds_dwordx4 v238, s[40:41] offset:1024\n\t"
      "s_add_u32 s40, s40, 7168\n\t"
      "s_addc_u32 s41, s41, 0\n\t"
      "global_load_lds_dwordx4 v238, s[40:41] offset:2048\n\t"
      "s_add_u32 s40, s40, 7168\n\t"
      "s_addc_u32 s41, s41, 0\n\t"
      "global_load_lds_dwordx4 v238, s[40:41] offset:3072\n\t"
      "s_branch Lge16_%=\n\t"
      "Lnd17_%=:\n\t"
      "s_cmp_eq_i32 s43, 0\n\t"
      "s_cbranch_scc1 Lnw17_%=\n\t"
      "s_setprio 0\n\t"
      "s_branch Lge17_%=\n\t"
      "Lnw17_%=:\n\t"
      "global_load_lds_dwordx4 v238, s[40:41] offset:0\n\t"
      "s_add_u32 s40, s40, 7168\n\t"
      "s_addc_u32 s41, s41, 0\n\t"
      "global_load_lds_dwordx4 v238, s[40:41] offset:1024\n\t"
      "s_add_u32 s40, s40, 7168\n\t"
      "s_addc_u32 s41, s41, 0\n\t"
      "global_load_lds_dwordx4 v238, s[40:41] offset:2048\n\t"
      "s_add_u32 s40, s40, 7168\n\t"
      "s_addc_u32 s41, s41, 0\n\t"
      "global_load_lds_dwordx4 v238, s[40:41] offset:3072\n\t"
      "s_branch Lge17_%=\n\t"
      "Lt1_%=:\n\t"
      "v_mfma_f32_32x32x16_bf16 v[18:33], v[176:179], v[204:207], v[18:33]\n\t"
      "v_exp_f32 v160, v128\n\t"
      "v_exp_f32 v161, v129\n\t"
      "v_mfma_f32_32x32x16_bf16 v[18:33], v[180:183], v[208:211], v[18:33]\n\t"
      "ds_read_b64_tr_b16 v[204:205], v55 offset:0\n\t"
      "ds_read_b64_tr_b16 v[206:207], v56 offset:0\n\t"
      "v_exp_f32 v162, v130\n\t"
      "v_exp_f32 v163, v131\n\t"
      "v_mfma_f32_32x32x16_bf16 v[144:159], v[200:203], v[38:41], 0\n\t"
      "v_add_f32 v160, 1.0, v160\n\t"
      "v_add_f32 v161, 1.0, v161\n\t"
      "v_add_f32 v162, 1.0, v162\n\t"
      "v_add_f32 v163, 1.0, v163\n\t"
      "v_mfma_f32_32x32x16_bf16 v[144:159], v[192:195], v[42:45], v[144:159]\n\t"
      "v_rcp_f32 v160, v160\n\t"
      "v_rcp_f32 v161, v161\n\t"
      "v_mfma_f32_32x32x16_bf16 v[144:159], v[196:199], v[46:49], v[144:159]\n\t"
      "v_rcp_f32 v162, v162\n\t"
      "v_rcp_f32 v163, v163\n\t"
      "v_mov_b64 v[178:179], 0\n\t"
      "v_mul_f32 v160, v128, v160\n\t"
      "v_mul_f32 v161, v129, v161\n\t"
      "v_mul_f32 v162, v130, v162\n\t"
      "v_mul_f32 v163, v131, v163\n\t"
      "v_cvt_pk_bf16_f32 v176, v160, v161\n\t"
      "v_cvt_pk_bf16_f32 v177, v162, v163\n\t"
      "s_waitcnt lgkmcnt(0)\n\t"
      "s_nop 2\n\t"
      "v_mfma_f32_32x32x16_bf16 v[2:17], v[176:179], v[204:207], v[2:17]\n\t"
      "v_exp_f32 v160, v144\n\t"
      "v_exp_f32 v161, v145\n\t"
      "v_exp_f32 v162, v146\n\t"
      "v_exp_f32 v163, v147\n\t"
      "v_pk_add_f32 v[160:161], v[160:161], 1.0 op_sel_hi:[1,0]\n\t"
      "v_pk_add_f32 v[162:163], v[162:163], 1.0 op_sel_hi:[1,0]\n\t"
      "v_rcp_f32 v160, v160\n\t"
      "v_rcp_f32 v161, v161\n\t"
      "v_rcp_f32 v162, v162\n\t"
      "v_rcp_f32 v163, v163\n\t"
      "s_nop 0\n\t"
      "v_pk_mul_f32 v[160:161], v[144:145], v[160:161]\n\t"
      "v_pk_mul_f32 v[162:163], v[146:147], v[162:163]\n\t"
      "v_cvt_pk_bf16_f32 v176, v160, v161\n\t"
      "v_cvt_pk_bf16_f32 v177, v162, v163\n\t"
      "s_nop 1\n\t"
      "v_mfma_f32_32x32x16_bf16 v[18:33], v[176:179], v[204:207], v[18:33]\n\t"
      "s_branch Ldone_%=\n\t"
      "Lt0_%=:\n\t"
      "v_mfma_f32_32x32x16_bf16 v[18:33], v[176:179], v[204:207], v[18:33]\n\t"
      "v_mfma_f32_32x32x16_bf16 v[18:33], v[180:183], v[208:211], v[18:33]\n\t"
      "s_branch Ldone_%=\n\t"
      "Lexit_even_%=:\n\t"
      "v_add_u32 v55, 0x400, v55\n\t"
      "v_add_u32 v56, 0x400, v56\n\t"
      "Ltail_%=:\n\t"
      "s_cmp_eq_u32 s46, 0\n\t"
      "s_cbranch_scc1 Lt0_%=\n\t"
      "s_cmp_eq_u32 s46, 1\n\t"
      "s_cbranch_scc1 Lt1_%=\n\t"
      "v_mfma_f32_32x32x16_bf16 v[18:33], v[176:179], v[204:207], v[18:33]\n\t"
      "v_exp_f32 v160, v128\n\t"
      "v_exp_f32 v161, v129\n\t"
      "v_exp_f32 v162, v130\n\t"
      "v_exp_f32 v163, v131\n\t"
      "v_mfma_f32_32x32x16_bf16 v[18:33], v[180:183], v[208:211], v[18:33]\n\t"
      "ds_read_b64_tr_b16 v[204:205], v55 offset:0\n\t"
      "ds_read_b64_tr_b16 v[206:207], v56 offset:0\n\t"
      "v_exp_f32 v164, v132\n\t"
      "v_exp_f32 v165, v133\n\t"
      "v_exp_f32 v166, v134\n\t"
      "v_exp_f32 v167, v135\n\t"
      "v_mfma_f32_32x32x16_bf16 v[144:159], v[200:203], v[38:41], 0\n\t"
      "v_add_f32 v160, 1.0, v160\n\t"
      "v_add_f32 v161, 1.0, v161\n\t"
      "v_add_f32 v162, 1.0, v162\n\t"
      "v_add_f32 v163, 1.0, v163\n\t"
      "v_add_f32 v164, 1.0, v164\n\t"
      "v_add_f32 v165, 1.0, v165\n\t"
      "v_add_f32 v166, 1.0, v166\n\t"
      "v_add_f32 v167, 1.0, v167\n\t"
      "v_mfma_f32_32x32x16_bf16 v[144:159], v[192:195], v[42:45], v[144:159]\n\t"
      "v_rcp_f32 v160, v160\n\t"
      "v_rcp_f32 v161, v161\n\t"
      "v_rcp_f32 v162, v162\n\t"
      "v_rcp_f32 v163, v163\n\t"
      "v_mfma_f32_32x32x16_bf16 v[144:159], v[196:199], v[46:49], v[144:159]\n\t"
      "v_rcp_f32 v164, v164\n\t"
      "v_rcp_f32 v165, v165\n\t"
      "v_rcp_f32 v166, v166\n\t"
      "v_rcp_f32 v167, v167\n\t"
      "v_mul_f32 v160, v128, v160\n\t"
      "v_mul_f32 v161, v129, v161\n\t"
      "v_mul_f32 v162, v130, v162\n\t"
      "v_mul_f32 v163, v131, v163\n\t"
      "v_mul_f32 v164, v132, v164\n\t"
      "v_mul_f32 v165, v133, v165\n\t"
      "v_mul_f32 v166, v134, v166\n\t"
      "v_mul_f32 v167, v135, v167\n\t"
      "v_cvt_pk_bf16_f32 v176, v160, v161\n\t"
      "v_cvt_pk_bf16_f32 v177, v162, v163\n\t"
      "v_cvt_pk_bf16_f32 v178, v164, v165\n\t"
      "v_cvt_pk_bf16_f32 v179, v166, v167\n\t"
      "s_waitcnt lgkmcnt(0)\n\t"
      "s_nop 1\n\t"
      "v_mfma_f32_32x32x16_bf16 v[2:17], v[176:179], v[204:207], v[2:17]\n\t"
      "v_exp_f32 v160, v144\n\t"
      "v_exp_f32 v161, v145\n\t"
      "v_exp_f32 v162, v146\n\t"
      "v_exp_f32 v163, v147\n\t"
      "v_exp_f32 v164, v148\n\t"
      "v_exp_f32 v165, v149\n\t"
      "v_exp_f32 v166, v150\n\t"
      "v_exp_f32 v167, v151\n\t"
      "v_pk_add_f32 v[160:161], v[160:161], 1.0 op_sel_hi:[1,0]\n\t"
      "v_pk_add_f32 v[162:163], v[162:163], 1.0 op_sel_hi:[1,0]\n\t"
      "v_pk_add_f32 v[164:165], v[164:165], 1.0 op_sel_hi:[1,0]\n\t"
      "v_pk_add_f32 v[166:167], v[166:167], 1.0 op_sel_hi:[1,0]\n\t"
      "v_rcp_f32 v160, v160\n\t"
      "v_rcp_f32 v161, v161\n\t"
      "v_rcp_f32 v162, v162\n\t"
      "v_rcp_f32 v163, v163\n\t"
      "v_rcp_f32 v164, v164\n\t"
      "v_rcp_f32 v165, v165\n\t"
      "v_rcp_f32 v166, v166\n\t"
      "v_rcp_f32 v167, v167\n\t"
      "s_nop 0\n\t"
      "v_pk_mul_f32 v[160:161], v[144:145], v[160:161]\n\t"
      "v_pk_mul_f32 v[162:163], v[146:147], v[162:163]\n\t"
      "v_pk_mul_f32 v[164:165], v[148:149], v[164:165]\n\t"
      "v_pk_mul_f32 v[166:167], v[150:151], v[166:167]\n\t"
      "v_cvt_pk_bf16_f32 v176, v160, v161\n\t"
      "v_cvt_pk_bf16_f32 v177, v162, v163\n\t"
      "v_cvt_pk_bf16_f32 v178, v164, v165\n\t"
      "v_cvt_pk_bf16_f32 v179, v166, v167\n\t"
      "s_nop 1\n\t"
      "v_mfma_f32_32x32x16_bf16 v[18:33], v[176:179], v[204:207], v[18:33]\n\t"
      "Ldone_%=:\n\t"
      "s_setprio 0\n\t"
      "s_waitcnt vmcnt(0) lgkmcnt(0)\n\t"
      "ds_read_b64_tr_b16 v[208:209], v239 offset:0\n\t"
      "ds_read_b64_tr_b16 v[210:211], v239 offset:128\n\t"
      "ds_read_b64_tr_b16 v[212:213], v239 offset:1024\n\t"
      "ds_read_b64_tr_b16 v[214:215], v239 offset:1152\n\t"
      "ds_read_b64_tr_b16 v[216:217], v239 offset:2048\n\t"
      "ds_read_b64_tr_b16 v[218:219], v239 offset:2176\n\t"
      "ds_read_b64_tr_b16 v[220:221], v239 offset:3072\n\t"
      "ds_read_b64_tr_b16 v[222:223], v239 offset:3200\n\t"
      "v_cvt_pk_bf16_f32 v192, v2, v3\n\t"
      "v_cvt_pk_bf16_f32 v193, v4, v5\n\t"
      "v_cvt_pk_bf16_f32 v194, v6, v7\n\t"
      "v_cvt_pk_bf16_f32 v195, v8, v9\n\t"
      "v_cvt_pk_bf16_f32 v196, v10, v11\n\t"
      "v_cvt_pk_bf16_f32 v197, v12, v13\n\t"
      "v_cvt_pk_bf16_f32 v198, v14, v15\n\t"
      "v_cvt_pk_bf16_f32 v199, v16, v17\n\t"
      "s_mov_b64 exec, s[96:97]\n\t"
      "ds_write_b64 v59, v[192:193] offset:0\n\t"
      "ds_write_b64 v59, v[194:195] offset:16\n\t"
      "ds_write_b64 v59, v[196:197] offset:32\n\t"
      "ds_write_b64 v59, v[198:199] offset:48\n\t"
      "v_cvt_pk_bf16_f32 v200, v18, v19\n\t"
      "v_cvt_pk_bf16_f32 v201, v20, v21\n\t"
      "v_cvt_pk_bf16_f32 v202, v22, v23\n\t"
      "v_cvt_pk_bf16_f32 v203, v24, v25\n\t"
      "v_cvt_pk_bf16_f32 v204, v26, v27\n\t"
      "v_cvt_pk_bf16_f32 v205, v28, v29\n\t"
      "v_cvt_pk_bf16_f32 v206, v30, v31\n\t"
      "v_cvt_pk_bf16_f32 v207, v32, v33\n\t"
      "ds_write_b64 v59, v[200:201] offset:64\n\t"
      "ds_write_b64 v59, v[202:203] offset:80\n\t"
      "ds_write_b64 v59, v[204:205] offset:96\n\t"
      "ds_write_b64 v59, v[206:207] offset:112\n\t"
      "s_mov_b64 exec, -1\n\t"
      "s_waitcnt lgkmcnt(0)\n\t"
      "s_barrier\n\t"
      "ds_read_b128 v[128:131], v64 offset:0\n\t"
      "ds_read_b128 v[132:135], v64 offset:4352\n\t"
      "ds_read_b128 v[136:139], v64 offset:8704\n\t"
      "ds_read_b128 v[140:143], v64 offset:64\n\t"
      "ds_read_b128 v[144:147], v64 offset:4416\n\t"
      "ds_read_b128 v[148:151], v64 offset:8768\n\t"
      "ds_read_b128 v[152:155], v64 offset:128\n\t"
      "ds_read_b128 v[156:159], v64 offset:4480\n\t"
      "ds_read_b128 v[160:163], v64 offset:8832\n\t"
      "ds_read_b128 v[164:167], v64 offset:192\n\t"
      "ds_read_b128 v[168:171], v64 offset:4544\n\t"
      "ds_read_b128 v[172:175], v64 offset:8896\n\t"
      "s_waitcnt lgkmcnt(9)\n\t"
      "v_mfma_f32_16x16x32_bf16 v[224:227], v[208:211], v[128:131], v[224:227]\n\t"
      "v_mfma_f32_16x16x32_bf16 v[228:231], v[208:211], v[132:135], v[228:231]\n\t"
      "v_mfma_f32_16x16x32_bf16 v[232:235], v[208:211], v[136:139], v[232:235]\n\t"
      "s_waitcnt lgkmcnt(6)\n\t"
      "v_mfma_f32_16x16x32_bf16 v[224:227], v[212:215], v[140:143], v[224:227]\n\t"
      "v_mfma_f32_16x16x32_bf16 v[228:231], v[212:215], v[144:147], v[228:231]\n\t"
      "v_mfma_f32_16x16x32_bf16 v[232:235], v[212:215], v[148:151], v[232:235]\n\t"
      "s_waitcnt lgkmcnt(3)\n\t"
      "v_mfma_f32_16x16x32_bf16 v[224:227], v[216:219], v[152:155], v[224:227]\n\t"
      "v_mfma_f32_16x16x32_bf16 v[228:231], v[216:219], v[156:159], v[228:231]\n\t"
      "v_mfma_f32_16x16x32_bf16 v[232:235], v[216:219], v[160:163], v[232:235]\n\t"
      "s_waitcnt lgkmcnt(0)\n\t"
      "v_mfma_f32_16x16x32_bf16 v[224:227], v[220:223], v[164:167], v[224:227]\n\t"
      "v_mfma_f32_16x16x32_bf16 v[228:231], v[220:223], v[168:171], v[228:231]\n\t"
      "v_mfma_f32_16x16x32_bf16 v[232:235], v[220:223], v[172:175], v[232:235]\n\t"
      "s_nop 5\n\t"
      "v_exp_f32 v192, v224\n\t"
      "v_exp_f32 v193, v225\n\t"
      "v_exp_f32 v194, v226\n\t"
      "v_exp_f32 v195, v227\n\t"
      "v_exp_f32 v196, v228\n\t"
      "v_exp_f32 v197, v229\n\t"
      "v_exp_f32 v198, v230\n\t"
      "v_exp_f32 v199, v231\n\t"
      "v_exp_f32 v200, v232\n\t"
      "v_exp_f32 v201, v233\n\t"
      "v_exp_f32 v202, v234\n\t"
      "v_exp_f32 v203, v235\n\t"
      "v_pk_fma_f32 v[192:193], v[192:193], s[80:81], s[80:81]\n\t"
      "v_pk_fma_f32 v[194:195], v[194:195], s[80:81], s[80:81]\n\t"
      "v_pk_fma_f32 v[196:197], v[196:197], s[80:81], s[80:81]\n\t"
      "v_pk_fma_f32 v[198:199], v[198:199], s[80:81], s[80:81]\n\t"
      "v_pk_fma_f32 v[200:201], v[200:201], s[80:81], s[80:81]\n\t"
      "v_pk_fma_f32 v[202:203], v[202:203], s[80:81], s[80:81]\n\t"
      "v_rcp_f32 v192, v192\n\t"
      "v_rcp_f32 v193, v193\n\t"
      "v_rcp_f32 v194, v194\n\t"
      "v_rcp_f32 v195, v195\n\t"
      "v_rcp_f32 v196, v196\n\t"
      "v_rcp_f32 v197, v197\n\t"
      "v_rcp_f32 v198, v198\n\t"
      "v_rcp_f32 v199, v199\n\t"
      "v_rcp_f32 v200, v200\n\t"
      "v_rcp_f32 v201, v201\n\t"
      "v_rcp_f32 v202, v202\n\t"
      "v_rcp_f32 v203, v203\n\t"
      "v_pk_mul_f32 v[192:193], v[224:225], v[192:193]\n\t"
      "v_pk_mul_f32 v[194:195], v[226:227], v[194:195]\n\t"
      "v_cvt_pk_bf16_f32 v128, v192, v193\n\t"
      "v_cvt_pk_bf16_f32 v129, v194, v195\n\t"
      "ds_write_b64 v65, v[128:129] offset:13056\n\t"
      "v_pk_mul_f32 v[196:197], v[228:229], v[196:197]\n\t"
      "v_pk_mul_f32 v[198:199], v[230:231], v[198:199]\n\t"
      "v_cvt_pk_bf16_f32 v130, v196, v197\n\t"
      "v_cvt_pk_bf16_f32 v131, v198, v199\n\t"
      "ds_write_b64 v65, v[130:131] offset:17408\n\t"
      "v_pk_mul_f32 v[200:201], v[232:233], v[200:201]\n\t"
      "v_pk_mul_f32 v[202:203], v[234:235], v[202:203]\n\t"
      "v_cvt_pk_bf16_f32 v132, v200, v201\n\t"
      "v_cvt_pk_bf16_f32 v133, v202, v203\n\t"
      "ds_write_b64 v65, v[132:133] offset:21760\n\t"
      "s_waitcnt lgkmcnt(0)\n\t"
      "s_barrier\n\t"
      "ds_read_b128 v[128:131], v66\n\t"
      "s_cmp_gt_u32 s48, 1\n\t"
      "s_cbranch_scc1 Lo_%=\n\t"
      "ds_read_b128 v[132:135], v66 offset:8704\n\t"
      "s_add_u32 s93, s92, 2097152\n\t"
      "s_waitcnt lgkmcnt(0)\n\t"
      "buffer_store_dwordx4 v[132:135], v67, s[88:91], s93 offen sc0 sc1\n\t"
      "Lo_%=:\n\t"
      "s_waitcnt lgkmcnt(0)\n\t"
      "buffer_store_dwordx4 v[128:131], v67, s[88:91], s92 offen sc0 sc1\n\t"
      "s_endpgm\n\t"
      "Lslow_%=:\n\t"
      "s_waitcnt vmcnt(0) lgkmcnt(0)\n\t"
      "s_barrier\n\t"
      "s_mov_b32 %[slow], 1\n\t"
      "Lend_%=:\n\t"
      "s_mov_b32 m0, s47\n\t"
      "s_nop 1\n\t"
      : [slow] "=&s"(slow), [ka] "+s"(ka)
      : [b] "s"(b), [r] "s"(b), "{v0}"(tid)
      : "memory", "scc", "vcc", "v2", "v3", "v4", "v5", "v6", "v7", "v8", "v9", "v10", "v11", "v12", "v13", "v14", "v15", "v16", "v17", "v18", "v19", "v20", "v21", "v22", "v23", "v24", "v25", "v26", "v27", "v28", "v29", "v30", "v31", "v32", "v33", "v34", "v35", "v36", "v37", "v38", "v39", "v40", "v41", "v42", "v43", "v44", "v45", "v46", "v47", "v48", "v49", "v50", "v51", "v52", "v53", "v54", "v55", "v56", "v57", "v58", "v59", "v60", "v61", "v62", "v63", "v64", "v65", "v66", "v67", "v68", "v69", "v70", "v71", "v72", "v73", "v74", "v75", "v76", "v77", "v78", "v79", "v80", "v81", "v82", "v83", "v84", "v85", "v86", "v87", "v88", "v89", "v90", "v91", "v92", "v93", "v94", "v95", "v96", "v97", "v98", "v99", "v100", "v101", "v102", "v103", "v104", "v105", "v106", "v107", "v108", "v109", "v110", "v111", "v112", "v113", "v114", "v115", "v116", "v117", "v118", "v119", "v120", "v121", "v122", "v123", "v124", "v125", "v126", "v127", "v128", "v129", "v130", "v131", "v132", "v133", "v134", "v135", "v136", "v137", "v138", "v139", "v140", "v141", "v142", "v143", "v144", "v145", "v146", "v147", "v148", "v149", "v150", "v151", "v152", "v153", "v154", "v155", "v156", "v157", "v158", "v159", "v160", "v161", "v162", "v163", "v164", "v165", "v166", "v167", "v168", "v169", "v170", "v171", "v172", "v173", "v174", "v175", "v176", "v177", "v178", "v179", "v180", "v181", "v182", "v183", "v184", "v185", "v186", "v187", "v188", "v189", "v190", "v191", "v192", "v193", "v194", "v195", "v196", "v197", "v198", "v199", "v200", "v201", "v202", "v203", "v204", "v205", "v206", "v207", "v208", "v209", "v210", "v211", "v212", "v213", "v214", "v215", "v216", "v217", "v218", "v219", "v220", "v221", "v222", "v223", "v224", "v225", "v226", "v227", "v228", "v229", "v230", "v231", "v232", "v233", "v234", "v235", "v236", "v237", "v238", "v239", "v240", "v241", "v242", "v243", "v244", "v245", "v246", "v247", "v248", "v249", "v250", "v251", "v252", "v253", "v254", "v255", "s12", "s13", "s14", "s15", "s16", "s17", "s18", "s19", "s20", "s21", "s22", "s23", "s24", "s25", "s26", "s27", "s28", "s29", "s30", "s31", "s36", "s37", "s38", "s39", "s40", "s41", "s42", "s43", "s44", "s45", "s46", "s47", "s48", "s49", "s50", "s51", "s52", "s53", "s54", "s55", "s56", "s57", "s58", "s59", "s60", "s61", "s62", "s63", "s64", "s65", "s66", "s67", "s68", "s69", "s70", "s71", "s72", "s73", "s74", "s75", "s76", "s77", "s78", "s79", "s80", "s81", "s82", "s83", "s84", "s85", "s86", "s87", "s88", "s89", "s90", "s91", "s92", "s93", "s94", "s95", "s96", "s97", "s98", "s99");
  if (slow) {
    void* const __attribute__((address_space(4)))* arg = (void* const __attribute__((address_space(4)))*)ka;
    standalone(lds, (const unsigned short*)arg[0], (const unsigned short*)arg[1], (const int*)arg[2], (const int*)arg[3],
               (const unsigned short*)arg[4], (const unsigned short*)arg[5], (unsigned short*)arg[7]);
  }
}

}

extern "C" void kernel_launch(void* const* d_in, const int* in_sizes, int n_in, void* d_out, int out_size, void* d_ws,
                              size_t ws_size, hipStream_t stream) {
  mpnn_layer<<<256, 512, 0, stream>>>((const unsigned short*)d_in[0], (const unsigned short*)d_in[1], (const int*)d_in[2],
                                      (const int*)d_in[3], (const unsigned short*)d_in[4], (const unsigned short*)d_in[5],
                                      (char*)d_ws, (unsigned short*)d_out);
}
```
